```python
import torch

def _softmax(x, axis=-1):
    return torch.softmax(x, dim=axis)
def _gelu(x):
    return torch.nn.functional.gelu(x, approximate="tanh")
def _silu(x):
    return torch.nn.functional.silu(x)
def _sigmoid(x):
    return torch.sigmoid(x)
def _layernorm(x, axis=-1, eps=1e-5):
    m = x.mean(dim=axis, keepdim=True)
    v = x.var(dim=axis, keepdim=True, unbiased=False)
    return (x - m) / torch.sqrt(v + eps)
def _scatter_add(tgt, idx, src, axis=0):
    return tgt.clone().index_add_(axis, idx, src)
def _segment_sum(src, seg, n):
    out = torch.zeros((n,) + src.shape[1:], dtype=src.dtype)
    return out.index_add_(0, seg, src)
def _pad(x, pw):
    flat = tuple(v for pair in reversed(pw) for v in pair)
    return torch.nn.functional.pad(x, flat)

kernel_name = 'gated_mix__spectral_fnet__geglu_ffn__prompt'

def setup_inputs():

    _w = (lambda a: (lambda t: t.to(torch.bfloat16) if t.is_floating_point() else t)(torch.as_tensor(a)))
    def _A_setup():
        B, S, D = 1, 2048, 512
        ns = torch.arange(S)[:, None] * torch.arange(S)[None, :]
        nd = torch.arange(D)[:, None] * torch.arange(D)[None, :]
        Ws_re = torch.cos(-2.0 * torch.pi * ns / S).to(float)
        Ws_im = torch.sin(-2.0 * torch.pi * ns / S).to(float)
        Wd_re = torch.cos(-2.0 * torch.pi * nd / D).to(float)
        Wd_im = torch.sin(-2.0 * torch.pi * nd / D).to(float)
        return {
            "x": _w((lambda *s: torch.randn(*s, dtype=torch.bfloat16) * 0.05)(B, S, D)),
            "dft_seq_re": _w(Ws_re),
            "dft_seq_im": _w(Ws_im),
            "dft_hid_re": _w(Wd_re),
            "dft_hid_im": _w(Wd_im),
        }
    _A_in = _A_setup()

    def _B_setup():
        B, S, D, F = 1, 2048, 512, 1376
        return {
            "x": _w((lambda *s: torch.randn(*s, dtype=torch.bfloat16) * 0.05)(B, S, D)),
            "w_gate": _w((lambda *s: torch.randn(*s, dtype=torch.bfloat16) * 0.8)(D, F)),
            "w_up": _w((lambda *s: torch.randn(*s, dtype=torch.bfloat16) * 0.8)(D, F)),
            "w_down": _w((lambda *s: torch.randn(*s, dtype=torch.bfloat16) * 0.8)(F, D)),
        }
    _B_in = _B_setup()

    return {
        "A_x": _A_in["x"],
        "A_dft_seq_re": _A_in["dft_seq_re"],
        "A_dft_seq_im": _A_in["dft_seq_im"],
        "A_dft_hid_re": _A_in["dft_hid_re"],
        "A_dft_hid_im": _A_in["dft_hid_im"],
        "B_x": _B_in["x"],
        "B_w_gate": _B_in["w_gate"],
        "B_w_up": _B_in["w_up"],
        "B_w_down": _B_in["w_down"],
        "gate_w": _w((lambda *s: torch.randn(*s, dtype=torch.bfloat16) * 0.05)(512) * 16.0),
    }

def reference(A_x, A_dft_seq_re, A_dft_seq_im, A_dft_hid_re, A_dft_hid_im, B_x, B_w_gate, B_w_up, B_w_down, gate_w):
    def _A_ref(x, dft_seq_re, dft_seq_im, dft_hid_re, dft_hid_im):
        a = torch.einsum("bsd,ed->bse", x, dft_hid_re)
        b = torch.einsum("bsd,ed->bse", x, dft_hid_im)
        y = torch.einsum("ts,bse->bte", dft_seq_re, a) - torch.einsum("ts,bse->bte", dft_seq_im, b)
        return y

    def _B_ref(x, w_gate, w_up, w_down):
        return (_gelu(x @ w_gate) * (x @ w_up)) @ w_down

    x = A_x
    g = _sigmoid((x * gate_w).sum(-1, keepdims=True))
    a = _A_ref(x, A_dft_seq_re, A_dft_seq_im, A_dft_hid_re, A_dft_hid_im)
    b = _B_ref(x, B_w_gate, B_w_up, B_w_down)
    return g * a + (1 - g) * b
```

```cpp
#include <hip/hip_runtime.h>
#include <hip/hip_bf16.h>

typedef unsigned short u16;
typedef unsigned long long u64;
typedef __bf16 bf16x8 __attribute__((ext_vector_type(8)));
typedef __bf16 bf16x4 __attribute__((ext_vector_type(4)));
typedef __bf16 bf16x2 __attribute__((ext_vector_type(2)));
typedef float f32x4 __attribute__((ext_vector_type(4)));
typedef float f32x2 __attribute__((ext_vector_type(2)));
typedef unsigned u32x4 __attribute__((ext_vector_type(4)));
typedef unsigned u32x2 __attribute__((ext_vector_type(2)));
typedef __attribute__((address_space(1))) u64 gu64;
typedef __attribute__((address_space(1))) unsigned gu32;
#define LAS __attribute__((address_space(3)))
#define DI __device__ __forceinline__
#define SB() __builtin_amdgcn_sched_barrier(0)
#define BARRIER() do { asm volatile("" ::: "memory"); __builtin_amdgcn_s_barrier(); asm volatile("" ::: "memory"); } while (0)
#define WAIT_LGKM0() __builtin_amdgcn_s_waitcnt(0xC07F)
#define RLX_AGENT __ATOMIC_RELAXED, __HIP_MEMORY_SCOPE_AGENT
template <int V> struct IC { static constexpr int value = V; };
template <int I, int N, class Fn> DI void sfor(Fn&& fn) { if constexpr (I < N) { fn(IC<I>{}); sfor<I + 1, N>(fn); } }
#define LAM __attribute__((always_inline))
template <int N> DI void wait_vm() { asm volatile("s_waitcnt vmcnt(%0)" :: "n"(N) : "memory"); }

#define AUX_C 0
#define AUX_B 16
#define FY_STEP 6

constexpr int WS_E = 0, WS_FA = 4096, WS_FH = 12288, WS_G = 20480, WS_FY = 65536;
constexpr int WS_AB = 1 << 20, WS_HB = 5 << 20, WS_YP = 11 << 20, WS_END = 17 << 20;
constexpr int SPIN_CAP = 1 << 20;

#define STAMP(i) do {} while (0)

DI unsigned cvtpk(float lo, float hi) { f32x2 v = {lo, hi}; bf16x2 b = __builtin_convertvector(v, bf16x2); return __builtin_bit_cast(unsigned, b); }
DI float bflo(unsigned v) { return __builtin_bit_cast(float, v << 16); }
DI float bfhi(unsigned v) { return __builtin_bit_cast(float, v & 0xffff0000u); }
DI float geglu(float x, float u) {
  const float c1 = -2.302208198f, c2 = -0.1029432397f;
  float z = x * __builtin_fmaf(x * x, c2, c1);
  float r = __builtin_amdgcn_rcpf(1.0f + __builtin_amdgcn_exp2f(z));
  return x * u * r;
}
DI unsigned opq(unsigned a) { asm volatile("" : "+v"(a)); return a; }
DI int lane_id() { int l; asm volatile("v_mbcnt_lo_u32_b32 %0, -1, 0\n\tv_mbcnt_hi_u32_b32 %0, -1, %0" : "=v"(l)); return l; }
DI u64 uni64(u64 v) {
  unsigned lo = __builtin_amdgcn_readfirstlane((unsigned)v), hi = __builtin_amdgcn_readfirstlane((unsigned)(v >> 32));
  return ((u64)hi << 32) | lo;
}
DI void spin_flags(const gu64* p, bool active, u64 tag) {
  for (int s = 0; s < SPIN_CAP; ++s) {
    u64 v = tag;
    if (active) v = __hip_atomic_load(p, RLX_AGENT);
    if (__all(v == tag)) break;
    __builtin_amdgcn_s_sleep(1);
  }
}

__global__ __launch_bounds__(512, 2) void fused_kernel(
    const u16* __restrict__ X, const u16* __restrict__ Cr, const u16* __restrict__ Ci, const u16* __restrict__ Hr,
    const u16* __restrict__ Hi, const u16* __restrict__ Wg, const u16* __restrict__ Wu, const u16* __restrict__ Wd,
    const u16* __restrict__ GW, u16* __restrict__ out, char* __restrict__ ws) {
#if defined(__HIP_DEVICE_COMPILE__)
  extern __shared__ __attribute__((aligned(16))) char smem_[];
  LAS char* smem = (LAS char*)smem_;
  const int tid = threadIdx.x, lane = tid & 63;
  const int wid = __builtin_amdgcn_readfirstlane(tid >> 6);
  const int b = blockIdx.x, xcd = b & 7, idx = b >> 3;
  const int m1 = (xcd & 1) * 8 + (idx & 7), n1 = (xcd >> 1) * 4 + (idx >> 3);
  const int blk0 = 22 * (n1 >> 2) + 6 * (n1 & 3) - (n1 == 15 ? 2 : 0);
  const bool big = ((n1 & 3) != 3) && (n1 != 14);
  const int k1 = ((m1 & 7) + 2 * (n1 & 3)) & 7;
  const int ks = xcd & 3, tm = (xcd >> 2) * 8 + (idx >> 2), tn = idx & 3;
  const int k2 = ((tm + tn) & 3) * 4;
  const int k3 = ((tm + tn) & 3) * 11 + ((tm >> 2) & 1) * 5;
  const int wgl = xcd * 32 + idx;
  STAMP(0);

  if (wid >= 4) {
    const int lw = wid - 4;
    auto rX = __builtin_amdgcn_make_buffer_rsrc((void*)X, 0, 2048 * 512 * 2, 0x00020000);
    auto rH = __builtin_amdgcn_make_buffer_rsrc((void*)(n1 < 8 ? Hr : Hi), 0, 512 * 512 * 2, 0x00020000);
    auto rG = __builtin_amdgcn_make_buffer_rsrc((void*)Wg, 0, 512 * 1376 * 2, 0x00020000);
    auto rU = __builtin_amdgcn_make_buffer_rsrc((void*)Wu, 0, 512 * 1376 * 2, 0x00020000);
    const int l3 = lane >> 3, l7 = lane & 7;
    int vX[4], vH[2], vW0, vW1[2];
#pragma unroll
    for (int i = 0; i < 4; ++i) {
      const int v = 32 * lw + 8 * i + l3;
      const int s = 32 * lw + 16 * (i & 1) + 8 * (lane >> 5) + 4 * (i >> 1) + (l3 & 3);
      vX[i] = (m1 * 128 + s) * 1024 + ((l7 ^ ((v >> 1) & 7)) * 16);
    }
#pragma unroll
    for (int i = 0; i < 2; ++i) {
      const int r = 16 * lw + 8 * i + l3;
      vH[i] = ((n1 & 7) * 64 + r) * 1024 + ((l7 ^ ((r >> 1) & 7)) * 16);
      const int f = ((r >> 1) & 1) | (((r >> 3) & 1) << 1);
      vW1[i] = r * 2752 + (blk0 * 16 + 32) * 2 + (((l7 >> 1) ^ f) * 32) + (l7 & 1) * 16;
    }
    { const int r = 16 * lw + (lane >> 2), c = lane & 3;
      vW0 = r * 2752 + blk0 * 32 + (((c >> 1) ^ ((r >> 3) & 1)) * 32) + (c & 1) * 16; }
    auto issue_c0 = [&](int j) {
      const int kb = (j + k1) & 7; LAS char* R = smem + (j & 3) * 32768;
#pragma unroll
      for (int i = 0; i < 4; ++i) __builtin_amdgcn_raw_ptr_buffer_load_lds(rX, R + (lw * 4 + i) * 1024, 16, vX[i], kb * 128, 0, 0);
#pragma unroll
      for (int i = 0; i < 2; ++i) __builtin_amdgcn_raw_ptr_buffer_load_lds(rH, R + 16384 + (lw * 2 + i) * 1024, 16, vH[i], kb * 128, 0, 0);
      __builtin_amdgcn_raw_ptr_buffer_load_lds(rG, R + 24576 + lw * 1024, 16, vW0, kb * (64 * 2752), 0, 0);
      __builtin_amdgcn_raw_ptr_buffer_load_lds(rU, R + 28672 + lw * 1024, 16, vW0, kb * (64 * 2752), 0, 0);
    };
    auto issue_c1 = [&](int j) {
      const int kb = (j + k1) & 7; LAS char* R = smem + j * 16384;
#pragma unroll
      for (int i = 0; i < 2; ++i) {
        __builtin_amdgcn_raw_ptr_buffer_load_lds(rG, R + (lw * 2 + i) * 1024, 16, vW1[i], kb * (64 * 2752), 0, 0);
        __builtin_amdgcn_raw_ptr_buffer_load_lds(rU, R + 8192 + (lw * 2 + i) * 1024, 16, vW1[i], kb * (64 * 2752), 0, 0);
      }
    };
    issue_c0(0); issue_c0(1);
    auto rC = __builtin_amdgcn_make_buffer_rsrc((void*)(ks < 2 ? Cr : Ci), 0, 2048 * 2048 * 2, 0x00020000);
    auto rW = __builtin_amdgcn_make_buffer_rsrc((void*)ws, 0, WS_END, 0x00020000);
    auto rD = __builtin_amdgcn_make_buffer_rsrc((void*)Wd, 0, 1376 * 512 * 2, 0x00020000);
    int vC[4], vD[4];
#pragma unroll
    for (int i = 0; i < 4; ++i) {
      const int v = 32 * lw + 8 * i + l3;
      const int s = 32 * lw + 16 * (i & 1) + 8 * (lane >> 5) + 4 * (i >> 1) + (l3 & 3);
      vC[i] = (tm * 128 + s) * 4096 + (ks & 1) * 2048 + ((l7 ^ ((v >> 1) & 7)) * 16);
      const int r = 8 * i + l3;
      vD[i] = r * 1024 + (tn * 128 + (ks & 1) * 64) * 2 + (((l7 >> 1) ^ ((r >> 1) & 3)) * 32) + (l7 & 1) * 16;
    }
    const int vB = lane * 16 + ((lw >> 1) * 16 + (tn & 1) * 8 + (lw & 1) * 4) * 1024;
    const int sB = WS_AB + (((ks >> 1) * 2 + (tn >> 1)) * 64 + (ks & 1) * 32) * 16384;
    const int sHb = WS_HB + (tm * 2 + (ks >> 1)) * (43 * 4096);
    auto issue_A = [&](int st, int slot) {
      const int kk = (st + k2) & 15; LAS char* R = smem + (slot < 2 ? 131072 + slot * 16384 : (slot - 2) * 16384);
#pragma unroll
      for (int i = 0; i < 4; ++i) __builtin_amdgcn_raw_ptr_buffer_load_lds(rC, R + (lw * 4 + i) * 1024, 16, vC[i], kk * 128, 0, AUX_C);
    };
    auto issue_B = [&](int st, int slot) {
      const int kk = (st + k2) & 15; LAS char* R = smem + 32768 + slot * 16384;
#pragma unroll
      for (int i = 0; i < 4; ++i) __builtin_amdgcn_raw_ptr_buffer_load_lds(rW, R + (lw * 4 + i) * 1024, 16, vB + i * 1024, sB + kk * 32768, 0, AUX_B);
    };
    auto issue_T = [&](int d) {
      constexpr int TH[5] = {98304, 131072, 147456, 0, 16384}, TW[5] = {114688, 32768, 49152, 65536, 81920};
      int kq = k3 + 4 * d + lw; kq -= (kq >= 43) ? 43 : 0; kq -= (kq >= 43) ? 43 : 0;
#pragma unroll
      for (int i = 0; i < 4; ++i) {
        __builtin_amdgcn_raw_ptr_buffer_load_lds(rW, smem + TH[d % 5] + lw * 4096 + i * 1024, 16, lane * 16 + i * 1024, sHb + kq * 4096, 0, AUX_B);
        __builtin_amdgcn_raw_ptr_buffer_load_lds(rD, smem + TW[d % 5] + lw * 4096 + i * 1024, 16, vD[i], kq * 32768, 0, 0);
      }
    };
    wait_vm<8>(); BARRIER();
    const u64 tag = uni64(*(LAS volatile u64*)(smem + 131072));
#pragma unroll
    for (int j = 2; j < 8; ++j) { issue_c0(j); wait_vm<8>(); BARRIER(); }
    issue_c1(0); issue_c1(1); wait_vm<8>(); BARRIER();
    issue_c1(2); issue_c1(3); wait_vm<12>(); BARRIER();
    issue_c1(4); issue_c1(5); issue_c1(6); issue_c1(7); issue_A(0, 0); issue_A(1, 1);
    wait_vm<32>(); BARRIER();
    wait_vm<28>(); BARRIER();
    issue_A(2, 2); wait_vm<28>(); BARRIER();
    issue_A(3, 3); wait_vm<28>(); BARRIER();
    wait_vm<24>(); BARRIER();
    wait_vm<20>(); BARRIER();
    wait_vm<16>(); BARRIER();
    BARRIER();
    {
      const gu64* fa = (const gu64*)(ws + WS_FA) + ((((ks & 1) * 8 + l3) * 16 + (ks >> 1) * 8 + tn * 2 + ((lane >> 2) & 1)) * 4 + (lane & 3));
      spin_flags(fa, true, tag);
    }
    STAMP(1);
    issue_B(0, 0); issue_B(1, 1); issue_B(2, 2); issue_B(3, 3);
    wait_vm<12>(); BARRIER();
    wait_vm<8>(); BARRIER();
    issue_A(4, 0); issue_B(4, 0); wait_vm<12>(); BARRIER();
    issue_A(5, 1); issue_B(5, 1); wait_vm<16>(); BARRIER();
#pragma unroll 1
    for (int i = 2; i < 10; i += 4) {
      issue_A(i + 4, 2); issue_B(i + 4, 2); wait_vm<16>(); BARRIER();
      issue_A(i + 5, 3); issue_B(i + 5, 3); wait_vm<16>(); BARRIER();
      issue_A(i + 6, 0); issue_B(i + 6, 0); wait_vm<16>(); BARRIER();
      issue_A(i + 7, 1); issue_B(i + 7, 1); wait_vm<16>(); BARRIER();
    }
    const gu64* fh = (const gu64*)(ws + WS_FH) + ((tm * 16 + ((lane >> 1) & 15)) * 4 + (ks >> 1) * 2 + (lane & 1));
    u64 pv;
    asm volatile("global_load_dwordx2 %0, %1, off sc1" : "=v"(pv) : "v"(fh) : "memory");
    issue_A(14, 2); issue_B(14, 2); wait_vm<17>(); BARRIER();
    asm volatile("s_waitcnt vmcnt(8)" : "+v"(pv) :: "memory");
    if (!__all(pv == tag)) spin_flags(fh, true, tag);
    STAMP(2);
    issue_A(15, 3); issue_B(15, 3); issue_T(0); wait_vm<24>(); BARRIER();
    issue_T(1); wait_vm<24>(); BARRIER();
    issue_T(2); wait_vm<24>(); BARRIER();
    issue_T(3); BARRIER();
    wait_vm<24>(); BARRIER();
    wait_vm<16>(); BARRIER();
    issue_T(4); issue_T(5); wait_vm<24>(); BARRIER();
    issue_T(6); wait_vm<24>(); BARRIER();
    issue_T(7); wait_vm<24>(); BARRIER();
    issue_T(8); wait_vm<24>(); BARRIER();
    issue_T(9); wait_vm<24>(); BARRIER();
    issue_T(10); wait_vm<24>(); BARRIER();
    wait_vm<16>(); BARRIER();
    wait_vm<8>(); BARRIER();
    wait_vm<0>(); BARRIER();
    if (b == 0 && wid == 7) {
#pragma unroll 1
      for (int q = 0; q < 4; ++q) spin_flags((const gu64*)(ws + WS_FA) + (q * 64 + lane) * 4, true, tag);
      if (lane == 0) __hip_atomic_store((gu64*)(ws + WS_E), tag, RLX_AGENT);
    }
  } else {
    const int w = wid;
    const u64 tag = uni64(__hip_atomic_load((const gu64*)(ws + WS_E), RLX_AGENT)) * 6364136223846793005ull + 1442695040888963407ull;
    if (tid == 0) *(LAS volatile u64*)(smem + 131072) = tag;
    auto rW = __builtin_amdgcn_make_buffer_rsrc((void*)ws, 0, WS_END, 0x00020000);
    auto rd128 = [&](unsigned a) { return *(LAS const bf16x8*)(smem + a); };
    auto rdtr = [&](unsigned a, int d) {
      bf16x4 lo = __builtin_amdgcn_ds_read_tr16_b64_v4bf16((LAS bf16x4*)(smem + a));
      bf16x4 hi = __builtin_amdgcn_ds_read_tr16_b64_v4bf16((LAS bf16x4*)(smem + a + d));
      return __builtin_shufflevector(lo, hi, 0, 1, 2, 3, 4, 5, 6, 7);
    };
    auto mma = [&](bf16x8 a, bf16x8 bb, f32x4 c) { return __builtin_amdgcn_mfma_f32_16x16x32_bf16(a, bb, c, 0, 0, 0); };
    const int sHp = WS_HB + ((m1 * 2 + (w >> 1)) * 43 + (blk0 >> 1)) * 4096 + (w & 1) * 2048;
    bf16x8 xf[16][2];
    WAIT_LGKM0();
    BARRIER();
    STAMP(1);
    {
      constexpr int D = 4;
      const int i16 = lane & 15, g = lane >> 4, q = i16 >> 2, p = lane & 3;
      unsigned aX[2], aH[2], aW0[2];
#pragma unroll
      for (int s = 0; s < 2; ++s) {
        aH[s] = opq(i16 * 128 + (((4 * s + g) ^ ((i16 >> 1) & 7)) * 16)); aX[s] = opq(aH[s] + w * 4096);
        aW0[s] = opq((8 * g + q) * 64 + ((s ^ (g & 1)) * 32) + p * 8);
      }
      f32x4 ab[2][4] = {}, gu[4][2] = {};
      bf16x8 fr[8];
      auto rdF = [&](auto Fc) LAM {
        constexpr int F = decltype(Fc)::value, j = F >> 4, s = (F >> 3) & 1, f = F & 7, R = (j & 1) * 32768;
        if (F && (F & 31) == 0) {
#pragma unroll
          for (int t = 0; t < 2; ++t) { aX[t] = opq(aX[t] ^ 65536); aH[t] = opq(aH[t] ^ 65536); aW0[t] = opq(aW0[t] ^ 65536); }
        }
        if (f == 0) { xf[F >> 3][0] = rd128(aX[s] + R); xf[F >> 3][1] = rd128(aX[s] + R + 2048); }
        if constexpr (f < 4) fr[F & 7] = rd128(aH[s] + R + 16384 + f * 2048);
        else fr[F & 7] = rdtr(aW0[(f - 4) >> 1] + R + 24576 + (f & 1) * 4096 + s * 2048, 256);
      };
      sfor<0, D>(rdF);
      SB();
      sfor<0, 128>([&](auto Fc) LAM {
        constexpr int F = decltype(Fc)::value, f = F & 7;
        if constexpr (F + D < 128 && ((F + D) & 15) == 0) { BARRIER(); SB(); }
        if constexpr (f < 4) ab[0][f] = mma(xf[F >> 3][0], fr[f], ab[0][f]); else gu[f - 4][0] = mma(fr[f], xf[F >> 3][0], gu[f - 4][0]);
        SB();
        if constexpr (F + D < 128) rdF(IC<F + D>{});
        SB();
        if constexpr (f < 4) ab[1][f] = mma(xf[F >> 3][1], fr[f], ab[1][f]); else gu[f - 4][1] = mma(fr[f], xf[F >> 3][1], gu[f - 4][1]);
        SB();
      });
      WAIT_LGKM0(); BARRIER();
      STAMP(2);
      const unsigned sgn = n1 < 8 ? 0u : 0x80008000u;
      const int sA = WS_AB + (((n1 >> 2) * 64 + m1 * 4 + w) * 16 + (n1 & 3) * 4) * 1024;
#pragma unroll
      for (int c = 0; c < 4; ++c) {
        u32x4 v = {cvtpk(ab[0][c][0], ab[0][c][1]) ^ sgn, cvtpk(ab[0][c][2], ab[0][c][3]) ^ sgn,
                   cvtpk(ab[1][c][0], ab[1][c][1]) ^ sgn, cvtpk(ab[1][c][2], ab[1][c][3]) ^ sgn};
        __builtin_amdgcn_raw_buffer_store_b128(v, rW, lane * 16 + c * 1024, sA, 16);
      }
#pragma unroll
      for (int mb = 0; mb < 2; ++mb) {
        u32x4 v = {cvtpk(geglu(gu[0][mb][0], gu[1][mb][0]), geglu(gu[0][mb][1], gu[1][mb][1])),
                   cvtpk(geglu(gu[0][mb][2], gu[1][mb][2]), geglu(gu[0][mb][3], gu[1][mb][3])),
                   cvtpk(geglu(gu[2][mb][0], gu[3][mb][0]), geglu(gu[2][mb][1], gu[3][mb][1])),
                   cvtpk(geglu(gu[2][mb][2], gu[3][mb][2]), geglu(gu[2][mb][3], gu[3][mb][3]))};
        __builtin_amdgcn_raw_buffer_store_b128(v, rW, lane * 16 + mb * 1024, sHp, 16);
      }
      SB();
    }
    STAMP(3);
    {
      unsigned aW1[4];
      { const int l = lane_id(), g = l >> 4, q = (l & 15) >> 2, p = l & 3, fsw = (q >> 1) | ((g & 1) << 1);
#pragma unroll
        for (int c = 0; c < 4; ++c) aW1[c] = opq((8 * g + q) * 128 + ((c ^ fsw) * 32) + p * 8); }
      gu64* const fa = (gu64*)(ws + WS_FA) + ((m1 * 16 + n1) * 4 + w);
      auto chunk1 = [&](auto NPc) LAM {
        constexpr int NP = decltype(NPc)::value, NF = 2 * NP, NT = 16 * NF, D = 4;
        f32x4 gu[NF][2] = {};
        bf16x8 fr[8];
        auto rdF = [&](auto Fc) LAM {
          constexpr int F = decltype(Fc)::value, kq = F / NF, f = F % NF;
          if (F == 8 * NF) {
#pragma unroll
            for (int c = 0; c < NP; ++c) aW1[c] = opq(aW1[c] + 65536);
          }
          fr[F & 7] = rdtr(aW1[f >> 1] + ((kq >> 1) & 3) * 16384 + (f & 1) * 8192 + (kq & 1) * 4096, 512);
        };
        sfor<0, D>(rdF);
        SB();
        sfor<0, NT>([&](auto Fc) LAM {
          constexpr int F = decltype(Fc)::value, f = F % NF;
          if constexpr (F + D < NT && ((F + D) % (2 * NF)) == 0) { BARRIER(); SB(); }
          if (F == 10 * NF) { wait_vm<0>(); if (lane == 0) __hip_atomic_store(fa, tag, RLX_AGENT); SB(); }
          gu[f][0] = mma(fr[F & 7], xf[F / NF][0], gu[f][0]);
          SB();
          if constexpr (F + D < NT) rdF(IC<F + D>{});
          SB();
          gu[f][1] = mma(fr[F & 7], xf[F / NF][1], gu[f][1]);
          SB();
        });
        WAIT_LGKM0(); BARRIER();
        STAMP(4);
        if (NP == 2 && n1 == 3) {
          auto rGW = __builtin_amdgcn_make_buffer_rsrc((void*)GW, 0, 1024, 0x00020000);
          f32x4 z[2] = {};
#pragma unroll
          for (int kq = 0; kq < 16; ++kq) {
            bf16x8 gw = __builtin_bit_cast(bf16x8, __builtin_amdgcn_raw_buffer_load_b128(rGW, (lane >> 4) * 16 + (kq & 1) * 64, (((kq >> 1) + k1) & 7) * 128, 0));
            z[0] = mma(gw, xf[kq][0], z[0]); z[1] = mma(gw, xf[kq][1], z[1]);
          }
          if (lane < 16) {
#pragma unroll
            for (int mb = 0; mb < 2; ++mb) {
              float gv = __builtin_amdgcn_rcpf(1.0f + __builtin_amdgcn_exp2f(-1.4426950409f * z[mb][0]));
              __hip_atomic_store((gu32*)(ws + WS_G) + m1 * 128 + w * 32 + mb * 16 + lane, __builtin_bit_cast(unsigned, gv), RLX_AGENT);
            }
          }
        }
#pragma unroll
        for (int i = 0; i < NP / 2; ++i)
#pragma unroll
          for (int mb = 0; mb < 2; ++mb) {
            const f32x4 g0 = gu[4 * i][mb], u0 = gu[4 * i + 1][mb], g1 = gu[4 * i + 2][mb], u1 = gu[4 * i + 3][mb];
            u32x4 v = {cvtpk(geglu(g0[0], u0[0]), geglu(g0[1], u0[1])), cvtpk(geglu(g0[2], u0[2]), geglu(g0[3], u0[3])),
                       cvtpk(geglu(g1[0], u1[0]), geglu(g1[1], u1[1])), cvtpk(geglu(g1[2], u1[2]), geglu(g1[3], u1[3]))};
            __builtin_amdgcn_raw_buffer_store_b128(v, rW, lane * 16 + mb * 1024 + (i + 1) * 4096, sHp, 16);
          }
        SB();
      };
      if (big) chunk1(IC<4>{}); else chunk1(IC<2>{});
    }
    STAMP(5);
    const int wm = w >> 1, wn = w & 1;
    const int lane = lane_id(), i16 = lane & 15, g = lane >> 4, q = i16 >> 2, p = lane & 3;
    {
      unsigned aA[2], aAh[2];
#pragma unroll
      for (int s = 0; s < 2; ++s) { aA[s] = opq(i16 * 128 + (((4 * s + g) ^ ((i16 >> 1) & 7)) * 16) + wm * 8192); aAh[s] = opq(aA[s] + 131072); }
      const unsigned aB = opq(lane * 16 + wn * 4096 + 32768);
      f32x4 acc[4][4] = {};
      bf16x8 fa[2][4], fb[2][4];
      auto rdA = [&](int set, int mb, int slot, int s) { fa[set][mb] = rd128((slot < 2 ? aAh[s] + slot * 16384 : aA[s] + (slot - 2) * 16384) + mb * 2048); };
      auto rdB = [&](int set, int nb, int slot, int s) { fb[set][nb] = rd128(aB + slot * 16384 + s * 8192 + nb * 1024); };
      auto mm = [&](int set, int nb, int mb) { acc[nb][mb] = mma(fb[set][nb], fa[set][mb], acc[nb][mb]); };
      gu64* const fh = (gu64*)(ws + WS_FH) + ((m1 * 16 + n1) * 4 + w);
      BARRIER();
      STAMP(6);
#pragma unroll
      for (int i = 0; i < 4; ++i) { rdA(0, i, 0, 0); rdB(0, i, 0, 0); }
      SB();
#pragma unroll 1
      for (int it = 0; it < 4; ++it) {
        if (it == 1) { wait_vm<0>(); if (lane == 0) __hip_atomic_store(fh, tag, RLX_AGENT); }
#pragma unroll
        for (int u = 0; u < 4; ++u) {
#pragma unroll
          for (int s = 0; s < 2; ++s) {
            const int cur = s, nxt = s ^ 1, ns = s ^ 1, nslot = s ? ((u + 1) & 3) : u;
            if (s == 1) { WAIT_LGKM0(); BARRIER(); SB(); }
            mm(cur, 0, 0); SB(); rdA(nxt, 0, nslot, ns); SB();
            mm(cur, 1, 0); SB(); rdB(nxt, 0, nslot, ns); SB();
            mm(cur, 2, 0); SB(); rdB(nxt, 1, nslot, ns); SB();
            mm(cur, 3, 0); SB(); rdB(nxt, 2, nslot, ns); SB();
            mm(cur, 0, 1); SB(); rdB(nxt, 3, nslot, ns); SB();
            mm(cur, 1, 1); SB(); rdA(nxt, 1, nslot, ns); SB();
            mm(cur, 2, 1); SB(); rdA(nxt, 2, nslot, ns); SB();
            mm(cur, 3, 1); SB(); rdA(nxt, 3, nslot, ns); SB();
            mm(cur, 0, 2); SB(); mm(cur, 1, 2); SB(); mm(cur, 2, 2); SB(); mm(cur, 3, 2); SB();
            mm(cur, 0, 3); SB(); mm(cur, 1, 3); SB(); mm(cur, 2, 3); SB(); mm(cur, 3, 3); SB();
          }
        }
      }
      STAMP(7);
      const int sY = WS_YP + wgl * 24576 + ((w - ks - 1) & 3) * 8192;
#pragma unroll
      for (int mb = 0; mb < 4; ++mb)
#pragma unroll
        for (int h = 0; h < 2; ++h) {
          u32x4 v = {cvtpk(acc[2 * h][mb][0], acc[2 * h][mb][1]), cvtpk(acc[2 * h][mb][2], acc[2 * h][mb][3]),
                     cvtpk(acc[2 * h + 1][mb][0], acc[2 * h + 1][mb][1]), cvtpk(acc[2 * h + 1][mb][2], acc[2 * h + 1][mb][3])};
          if (w != ks) __builtin_amdgcn_raw_buffer_store_b128(v, rW, lane * 16 + (mb * 2 + h) * 1024, sY, 16);
          else *(LAS u32x4*)(smem + 16384 + (mb * 2 + h) * 1024 + lane * 16) = v;
        }
      WAIT_LGKM0();
    }
    STAMP(8);
    {
      const unsigned aH0 = opq(lane * 16 + wm * 2048), aH1 = opq(aH0 + 98304);
      const int fsw = (q >> 1) | ((g & 1) << 1);
      unsigned aD0[2], aD1[2];
#pragma unroll
      for (int c = 0; c < 2; ++c) { aD0[c] = opq((4 * g + q) * 128 + (((2 * wn + c) ^ fsw) * 32) + p * 8 + 32768); aD1[c] = opq(aD0[c] + 81920); }
      f32x4 acc[2][2] = {};
      u32x4 y[4][2];
      gu64* const fy = (gu64*)(ws + WS_FY);
      auto step = [&](int sl, int nk) {
        const unsigned ah = sl == 0 ? aH1 : sl == 1 ? aH1 + 32768 : sl == 2 ? aH1 + 49152 : sl == 3 ? aH0 : aH0 + 16384;
        const unsigned ad0 = sl == 0 ? aD1[0] : aD0[0] + (sl - 1) * 16384, ad1 = sl == 0 ? aD1[1] : aD0[1] + (sl - 1) * 16384;
        bf16x8 hf[4][2], df[4][2];
#pragma unroll
        for (int j = 0; j < nk; ++j) {
          hf[j][0] = rd128(ah + j * 4096); df[j][0] = rdtr(ad0 + j * 4096, 2048);
          df[j][1] = rdtr(ad1 + j * 4096, 2048); hf[j][1] = rd128(ah + j * 4096 + 1024);
        }
        SB();
#pragma unroll
        for (int j = 0; j < nk; ++j) {
          acc[0][0] = mma(df[j][0], hf[j][0], acc[0][0]); acc[1][0] = mma(df[j][1], hf[j][0], acc[1][0]);
          acc[0][1] = mma(df[j][0], hf[j][1], acc[0][1]); acc[1][1] = mma(df[j][1], hf[j][1], acc[1][1]);
        }
        SB();
      };
      BARRIER();
      y[3][0] = *(LAS const u32x4*)(smem + 16384 + (wm * 4 + wn) * 1024 + lane * 16);
      y[3][1] = *(LAS const u32x4*)(smem + 16384 + (wm * 4 + 2 + wn) * 1024 + lane * 16);
      asm volatile("s_waitcnt lgkmcnt(0)" : "+v"(y[3][0]), "+v"(y[3][1]) :: "memory");
#pragma unroll 1
      for (int it = 0; it < 2; ++it) {
#pragma unroll
        for (int sl = 0; sl < 5; ++sl) {
          if (it * 5 + sl == FY_STEP && w != ks) { wait_vm<0>(); if (lane == 0) __hip_atomic_store(fy + (wgl * 4 + w) * 8, tag, RLX_AGENT); }
          step(sl, 4);
          BARRIER();
        }
      }
      step(0, 3);
      STAMP(9);
      spin_flags(fy + ((((xcd & 4) | (lane & 3)) * 32 + idx) * 4 + ks) * 8, lane < 4 && lane != ks, tag);
      STAMP(10);
      float gv[2];
#pragma unroll
      for (int mb = 0; mb < 2; ++mb)
        gv[mb] = __builtin_bit_cast(float, __hip_atomic_load((gu32*)(ws + WS_G) + tm * 128 + (ks >> 1) * 64 + wm * 32 + mb * 16 + i16, RLX_AGENT));
#pragma unroll
      for (int o = 0; o < 3; ++o) {
        const int so = (ks - o - 1) & 3;
        const int sY = WS_YP + ((((xcd & 4) | so) * 32 + idx) * 3 + o) * 8192 + (wm * 4 + wn) * 1024;
        y[o][0] = __builtin_amdgcn_raw_buffer_load_b128(rW, lane * 16, sY, 16);
        y[o][1] = __builtin_amdgcn_raw_buffer_load_b128(rW, lane * 16 + 2048, sY, 16);
      }
#pragma unroll
      for (int mb = 0; mb < 2; ++mb) {
        const int row = tm * 128 + (ks >> 1) * 64 + wm * 32 + 8 * q + 4 * mb + p;
        u16* op = out + row * 512 + tn * 128 + (ks & 1) * 64 + wn * 32 + 4 * g;
        const float gg = gv[mb], gn = 1.0f - gg;
#pragma unroll
        for (int nb = 0; nb < 2; ++nb) {
          float r[4];
#pragma unroll
          for (int e = 0; e < 2; ++e) {
            const float lo = (bflo(y[0][mb][nb * 2 + e]) + bflo(y[1][mb][nb * 2 + e])) + (bflo(y[2][mb][nb * 2 + e]) + bflo(y[3][mb][nb * 2 + e]));
            const float hi = (bfhi(y[0][mb][nb * 2 + e]) + bfhi(y[1][mb][nb * 2 + e])) + (bfhi(y[2][mb][nb * 2 + e]) + bfhi(y[3][mb][nb * 2 + e]));
            r[2 * e] = gg * lo + gn * acc[nb][mb][2 * e];
            r[2 * e + 1] = gg * hi + gn * acc[nb][mb][2 * e + 1];
          }
          u32x2 v = {cvtpk(r[0], r[1]), cvtpk(r[2], r[3])};
          *(u32x2*)(op + nb * 16) = v;
        }
      }
      STAMP(11);
    }
  }
#endif
}

extern "C" void kernel_launch(void* const* d_in, const int* in_sizes, int n_in, void* d_out, int out_size, void* d_ws,
                              size_t ws_size, hipStream_t stream) {
  (void)in_sizes; (void)n_in; (void)out_size; (void)ws_size;
  constexpr int kLds = 163840;
  (void)hipFuncSetAttribute((const void*)fused_kernel, hipFuncAttributeMaxDynamicSharedMemorySize, kLds);
  fused_kernel<<<256, 512, kLds, stream>>>((const u16*)d_in[0], (const u16*)d_in[1], (const u16*)d_in[2], (const u16*)d_in[3],
                                           (const u16*)d_in[4], (const u16*)d_in[6], (const u16*)d_in[7], (const u16*)d_in[8],
                                           (const u16*)d_in[9], (u16*)d_out, (char*)d_ws);
}
```

```cpp
#include <hip/hip_runtime.h>
#include <hip/hip_bf16.h>

typedef unsigned short u16;
typedef unsigned long long u64;
typedef __bf16 bf16x8 __attribute__((ext_vector_type(8)));
typedef __bf16 bf16x4 __attribute__((ext_vector_type(4)));
typedef __bf16 bf16x2 __attribute__((ext_vector_type(2)));
typedef float f32x4 __attribute__((ext_vector_type(4)));
typedef float f32x2 __attribute__((ext_vector_type(2)));
typedef unsigned u32x4 __attribute__((ext_vector_type(4)));
typedef unsigned u32x2 __attribute__((ext_vector_type(2)));
typedef __attribute__((address_space(1))) u64 gu64;
typedef __attribute__((address_space(1))) unsigned gu32;
#define LAS __attribute__((address_space(3)))
#define DI __device__ __forceinline__
#define SB() __builtin_amdgcn_sched_barrier(0)
#define BARRIER() do { asm volatile("" ::: "memory"); __builtin_amdgcn_s_barrier(); asm volatile("" ::: "memory"); } while (0)
#define WAIT_LGKM0() __builtin_amdgcn_s_waitcnt(0xC07F)
#define RLX_AGENT __ATOMIC_RELAXED, __HIP_MEMORY_SCOPE_AGENT
template <int V> struct IC { static constexpr int value = V; };
template <int I, int N, class Fn> DI void sfor(Fn&& fn) { if constexpr (I < N) { fn(IC<I>{}); sfor<I + 1, N>(fn); } }
#define LAM __attribute__((always_inline))
template <int N> DI void wait_vm() { asm volatile("s_waitcnt vmcnt(%0)" :: "n"(N) : "memory"); }

#define K1MODE 2
#define FA_ST 3
#define PH 14
#define SH 1
#define SELH (tm & 3)
#define FY_STEP 3
#define FY_POLL 8
#define FY_CHK (FY_POLL + 1)
#define PA0 5
#define SA0 1
#define PA1 9
#define SA1 1
#define PA2 14
#define SA2 1
#define PA3 18
#define SA3 1
#define SPIN_SLEEP 1
#define SEL (tm & 3)
#define U0E 0
#define TWG 1
#define TWN 3
#define WSOFF 0
#define DSCHED 0, 1, 2, 4, 6, 8, 10, 11, 12, 14, 14
#define AUX_O 16
#define AUX_1 0

constexpr int WS_E = 0, WS_FA = 4096, WS_FH = 12288, WS_G = 20480, WS_FY = 65536;
constexpr int WS_AB = 1 << 20, WS_HB = 5 << 20, WS_YP = 11 << 20, WS_END = 17 << 20;
constexpr int SPIN_CAP = 1 << 20;

#define STAMP(i) do {} while (0)
#define STAMP_OUT() do {} while (0)

#define LBAR() BARRIER()
#define FS(n) do {} while (0)

DI unsigned cvtpk(float lo, float hi) { f32x2 v = {lo, hi}; bf16x2 b = __builtin_convertvector(v, bf16x2); return __builtin_bit_cast(unsigned, b); }
DI float bflo(unsigned v) { return __builtin_bit_cast(float, v << 16); }
DI float bfhi(unsigned v) { return __builtin_bit_cast(float, v & 0xffff0000u); }
DI float geglu(float x, float u) {
  const float c1 = -2.302208198f, c2 = -0.1029432397f;
  float z = x * __builtin_fmaf(x * x, c2, c1);
  float r = __builtin_amdgcn_rcpf(1.0f + __builtin_amdgcn_exp2f(z));
  return x * u * r;
}
DI void mfa(f32x4& c, bf16x8 a, bf16x8 b) { asm volatile("v_mfma_f32_16x16x32_bf16 %0, %1, %2, %0" : "+v"(c) : "v"(a), "v"(b)); }
DI void mf0(f32x4& c, bf16x8 a, bf16x8 b) { asm volatile("v_mfma_f32_16x16x32_bf16 %0, %1, %2, 0" : "=v"(c) : "v"(a), "v"(b)); }
template <bool FIRST> DI void mfx(f32x4& c, bf16x8 a, bf16x8 b) { if constexpr (FIRST) mf0(c, a, b); else mfa(c, a, b); }
DI void settle() { asm volatile("s_nop 7"); }
DI void pin(f32x4& c) { asm volatile("" : "+v"(c)); }
DI unsigned opq(unsigned a) { asm volatile("" : "+v"(a)); return a; }
DI int lane_id() { int l; asm volatile("v_mbcnt_lo_u32_b32 %0, -1, 0\n\tv_mbcnt_hi_u32_b32 %0, -1, %0" : "=v"(l)); return l; }
DI u64 uni64(u64 v) {
  unsigned lo = __builtin_amdgcn_readfirstlane((unsigned)v), hi = __builtin_amdgcn_readfirstlane((unsigned)(v >> 32));
  return ((u64)hi << 32) | lo;
}
constexpr int SLOT = 36864;
constexpr int wslot(int j) { return (j & 3) == 0 ? 2 * SLOT + 8192 : (j & 3) == 1 ? 8192 : (j & 3) == 2 ? SLOT + 8192 : 131072; }
DI void spin_flags(const gu64* p, bool active, u64 tag) {
#pragma unroll 1
  for (int s = 0; s < SPIN_CAP; ++s) {
    u64 v = tag;
    if (active) v = __hip_atomic_load(p, RLX_AGENT);
    if (__all(v == tag)) break;
    __builtin_amdgcn_s_sleep(SPIN_SLEEP);
  }
}

__global__ __launch_bounds__(512, 2) void fused_kernel(
    const u16* __restrict__ X, const u16* __restrict__ Cr, const u16* __restrict__ Ci, const u16* __restrict__ Hr,
    const u16* __restrict__ Hi, const u16* __restrict__ Wg, const u16* __restrict__ Wu, const u16* __restrict__ Wd,
    const u16* __restrict__ GW, u16* __restrict__ out, char* __restrict__ ws) {
#if defined(__HIP_DEVICE_COMPILE__)
  extern __shared__ __attribute__((aligned(16))) char smem_[];
  LAS char* smem = (LAS char*)smem_;
  const int tid = threadIdx.x, lane = tid & 63;
  const int wid = __builtin_amdgcn_readfirstlane(tid >> 6);
  const int b = blockIdx.x, xcd = b & 7, idx = b >> 3;
  const int m1 = (~xcd & 4) * 2 + (idx & 7), n1 = ((xcd & 3) ^ 2) * 4 + (idx >> 3);
  const int blk0 = 22 * (n1 >> 2) + 6 * (n1 & 3) - (n1 == 15 ? 2 : 0);
  const bool big = ((n1 & 3) != 3) && (n1 != 14);
  const int ks = xcd & 3, tm = (xcd >> 2) * 8 + (idx >> 2), tn = idx & 3;
  const int wgl = xcd * 32 + idx;
  constexpr int OOB = 0x40000000;
  STAMP(0);
  asm volatile("" : "+s"(X), "+s"(Cr), "+s"(Ci), "+s"(Hr), "+s"(Hi), "+s"(Wg), "+s"(Wu), "+s"(Wd), "+s"(GW), "+s"(out), "+s"(ws));

  if (__builtin_expect(wid >= 4, 1)) {
    const int lw = wid - 4;
    auto rX = __builtin_amdgcn_make_buffer_rsrc((void*)X, 0, 2048 * 512 * 2, 0x00020000);
    auto rH = __builtin_amdgcn_make_buffer_rsrc((void*)(n1 < 8 ? Hr : Hi), 0, 512 * 512 * 2, 0x00020000);
    auto rG = __builtin_amdgcn_make_buffer_rsrc((void*)Wg, 0, 512 * 1376 * 2, 0x00020000);
    auto rU = __builtin_amdgcn_make_buffer_rsrc((void*)Wu, 0, 512 * 1376 * 2, 0x00020000);
    const int l3 = lane >> 3, l7 = lane & 7;
    constexpr int KM = (1 << K1MODE) - 1;
    const int k1 = K1MODE == 3 ? (m1 & 7) + 2 * (n1 & 3) : m1 + n1;
    auto kblk = [&](int j) { return (j & ~KM) | ((j + k1) & KM); };
    int vX[4], vH[2], vW[3];
#pragma unroll
    for (int i = 0; i < 4; ++i) {
      const int v = 32 * lw + 8 * i + l3;
      const int s = 32 * lw + 16 * (i & 1) + 8 * (lane >> 5) + 4 * (i >> 1) + (l3 & 3);
      vX[i] = (m1 * 128 + s) * 1024 + ((l7 ^ ((v >> 1) & 7)) * 16);
    }
    auto issue_x = [&](int j) {
#pragma unroll
      for (int i = 0; i < 4; ++i)
        __builtin_amdgcn_raw_ptr_buffer_load_lds(rX, smem + j % 3 * SLOT + 20480 + (lw * 4 + i) * 1024, 16, vX[i], kblk(j) * 128, 0, AUX_1);
    };
    FS(0);
    issue_x(0);
    FS(1);
#pragma unroll
    for (int i = 0; i < 2; ++i) {
      const int r = 16 * lw + 8 * i + l3;
      vH[i] = ((n1 & 7) * 64 + r) * 1024 + ((l7 ^ ((r >> 1) & 7)) * 16);
    }
#pragma unroll
    for (int i = 0; i < 3; ++i) {
      const int e = i * 64 + lane, rr = (e * 171) >> 11, s = e - 12 * rr, c = (((s >> 1) ^ (rr >> 3)) << 1) | (s & 1);
      vW[i] = (!big && c >= 8) ? OOB : (16 * lw + rr) * 2752 + blk0 * 32 + c * 16;
    }
    auto issue_w = [&](int j) {
      LAS char* R = smem + j % 3 * SLOT;
#pragma unroll
      for (int i = 0; i < 2; ++i) __builtin_amdgcn_raw_ptr_buffer_load_lds(rH, R + (lw * 2 + i) * 1024, 16, vH[i], kblk(j) * 128, 0, AUX_1);
#pragma unroll
      for (int i = 0; i < 3; ++i) __builtin_amdgcn_raw_ptr_buffer_load_lds(rG, smem + wslot(j) + (lw * 3 + i) * 1024, 16, vW[i], kblk(j) * (64 * 2752), 0, AUX_1);
    };
    auto issue_u = [&](int j) {
      LAS char* R = smem + (j == 0 ? 147456 : j < 4 ? 61440 + j * 12288 : (j - 4) * 12288);
#pragma unroll
      for (int i = 0; i < 3; ++i) __builtin_amdgcn_raw_ptr_buffer_load_lds(rU, R + (lw * 3 + i) * 1024, 16, vW[i], kblk(j) * (64 * 2752), 0, AUX_1);
    };
    if (!big && lw == 0) {
      auto rGW = __builtin_amdgcn_make_buffer_rsrc((void*)GW, 0, 1024, 0x00020000);
      __builtin_amdgcn_raw_ptr_buffer_load_lds(rGW, smem + 159744, 16, kblk(l3) * 128 + l7 * 16, 0, 0, 0);
    }
    FS(2);
    issue_w(0); issue_x(1);
    FS(3);
    auto rC = __builtin_amdgcn_make_buffer_rsrc((void*)(ks < 2 ? Cr : Ci), 0, 2048 * 2048 * 2, 0x00020000);
    auto rW = __builtin_amdgcn_make_buffer_rsrc((void*)ws, 0, WS_END, 0x00020000);
    auto rD = __builtin_amdgcn_make_buffer_rsrc((void*)Wd, 0, 1376 * 512 * 2, 0x00020000);
    int vC[4], vD[4];
#pragma unroll
    for (int i = 0; i < 4; ++i) {
      const int v = 32 * lw + 8 * i + l3;
      const int s = 32 * lw + 16 * (i & 1) + 8 * (lane >> 5) + 4 * (i >> 1) + (l3 & 3);
      vC[i] = (tm * 128 + s) * 4096 + (ks & 1) * 2048 + ((l7 ^ ((v >> 1) & 7)) * 16);
      const int r = 8 * i + l3;
      vD[i] = r * 1024 + (tn * 128 + (ks & 1) * 64) * 2 + (((l7 >> 1) ^ ((r >> 1) & 3)) * 32) + (l7 & 1) * 16;
    }
    int vL = lane * 16;
    const int sB = WS_AB + (((ks >> 1) * 2 + (tn >> 1)) * 64 + (ks & 1) * 32) * 16384 + ((lw >> 1) * 16 + (tn & 1) * 8 + (lw & 1) * 4) * 1024;
    const int sHb = WS_HB + (tm * 2 + (ks >> 1)) * (43 * 4096);
    auto linear = [&](auto Nc, LAS char* R, int v, int so) LAM {
      sfor<0, decltype(Nc)::value>([&](auto Ic) LAM { __builtin_amdgcn_raw_ptr_buffer_load_lds(rW, R, 16, v, so, decltype(Ic)::value * 1024, 16); });
    };
    const int k2 = tn + 2 * ((tm >> 2) & 1), k3 = (11 * tn + ((43 * (tm & 7)) >> 3)) % 43 + lw;
    auto kk = [&](int st) { return (((st >> 2) + tm) & 3) * 4 + ((st + k2) & 3); };
    auto issue_A = [&](int st) {
      LAS char* R = smem + ((st & 3) == 1 ? 9 : (st & 3) == 2 ? 6 : (st & 3) == 3 ? 1 : st == 4 ? 8 : st == 8 ? 0 : 7) * 16384 + lw * 4096;
#pragma unroll
      for (int i = 0; i < 4; ++i) __builtin_amdgcn_raw_ptr_buffer_load_lds(rC, R + i * 1024, 16, vC[i], kk(st) * 128, 0, 0);
    };
    auto issue_B = [&](int st) { linear(IC<4>{}, smem + 32768 + ((st + 3) & 3) * 16384 + lw * 4096, vL, sB + kk(st) * 32768); };
    auto issue_T = [&](int d, int part) {
      const int sl = d % 5; int kq = k3 + 4 * d; kq -= kq >= 43 ? 43 : 0;
      if (part & 1) linear(IC<4>{}, smem + (sl == 0 ? 8 : sl == 1 ? 7 : sl == 2 ? 9 : sl == 3 ? 6 : 1) * 16384 + lw * 4096, vL, sHb + kq * 4096);
      LAS char* R = smem + (sl ? 32768 + ((sl + 2) & 3) * 16384 : 0) + lw * 4096;
#pragma unroll
      for (int i = 0; i < 4; ++i) if (part & 2) __builtin_amdgcn_raw_ptr_buffer_load_lds(rD, R + i * 1024, 16, vD[i], kq * 32768, 0, 0);
    };
    const int sel = SEL, selh = SELH;
    u32x4 hv[6];
    u64 tag;
    unsigned tch = 0;
    auto fin = [&](auto Kc, auto Lc, auto Nc) LAM {
      constexpr int K = decltype(Kc)::value, L = decltype(Lc)::value, N = decltype(Nc)::value;
      constexpr int P[4] = {PA0, PA1, PA2, PA3}, S[4] = {SA0, SA1, SA2, SA3};
      int n = 0;
      if constexpr (K >= PH && K <= PH + 3 * SH) {
        if (K == PH + SH * selh) {
          const int sHp = WS_HB + ((m1 * 2 + (lw >> 1)) * 43 + (blk0 >> 1)) * 4096 + (lw & 1) * 2048;
#pragma unroll
          for (int t = 0; t < 6; ++t)
            if (t < 4 || big) __builtin_amdgcn_raw_buffer_store_b128(hv[t], rW, vL + (t & 1) * 1024, sHp + (t >> 1) * 4096, 16);
        }
      }
      if constexpr (K >= PH && L <= PH + 3 * SH) n += PH + SH * selh >= L && PH + SH * selh <= K;
      sfor<0, 4>([&](auto Gc) LAM {
        constexpr int G = decltype(Gc)::value;
        if constexpr (K >= P[G] && K <= P[G] + 3 * S[G]) {
          if (K == P[G] + S[G] * sel) {
            constexpr int R = (42 + TWN) / TWN, r0 = (G - TWG) * R;
            if constexpr (G >= TWG && G < TWG + TWN)
              asm volatile("buffer_load_dword %0, %1, %2, 0 offen" : "+v"(tch) : "v"(lane < (r0 + R > 43 ? 43 - r0 : R) ? (idx * 43 + r0 + lane) * 1024 + lw * 256 + (ks & 1) * 128 : OOB), "s"(rD) : "memory");
            issue_A(4 * G);
          }
        }
        if constexpr (K >= P[G] && L <= P[G] + 3 * S[G]) n += P[G] + S[G] * sel >= L && P[G] + S[G] * sel <= K;
      });
      if constexpr (N >= 0) { if (n == 0) wait_vm<N>(); else if (n == 1) wait_vm<N + 4>(); else if (n == 2) wait_vm<N + 8>(); else wait_vm<N + 12>(); }
      if constexpr (L - 1 >= PH && L - 1 <= PH + 3 * SH && K > 16) {
        if (L - 1 == PH + SH * selh && lane == 0) __hip_atomic_store((gu64*)(ws + WS_FH) + ((m1 * 16 + n1) * 4 + lw), tag, RLX_AGENT);
      }
    };
    wait_vm<4>(); LBAR();
    if (U0E) issue_u(0);
    issue_w(1);
    const char* wse = ws;
    asm volatile("" : "+s"(wse));
    u64 epoch = *(const __attribute__((address_space(4))) u64*)(wse + WS_E);
    sfor<0, 6>([&](auto Kc) LAM {
      constexpr int k = decltype(Kc)::value;
      issue_x(k + 2); issue_w(k + 2);
      if (k == 0) asm volatile("" : "+s"(epoch));
      fin(Kc, IC<k - 1>{}, IC<9>{}); LBAR();
    });
    if (!U0E) issue_u(0);
    issue_u(1); fin(IC<6>{}, IC<5>{}, IC<U0E ? 3 : 6>{}); LBAR();
    issue_u(2); issue_u(3); issue_u(4); fin(IC<7>{}, IC<6>{}, IC<12>{}); LBAR();
    tag = epoch * 6364136223846793005ull + 1442695040888963407ull;
    const gu64* fa = (const gu64*)(ws + WS_FA) + ((((ks & 1) * 8 + l3) * 16 + (ks >> 1) * 8 + tn * 2 + ((lane >> 2) & 1)) * 4 + (lane & 3));
    u64 pv;
    fin(IC<8>{}, IC<6>{}, IC<9>{}); LBAR();
    issue_u(5); issue_u(6); fin(IC<9>{}, IC<7>{}, IC<12>{}); LBAR();
    issue_u(7); fin(IC<10>{}, IC<7>{}, IC<9>{}); LBAR();
    issue_A(1); fin(IC<11>{}, IC<10>{}, IC<4>{}); LBAR();
    asm volatile("global_load_dwordx2 %0, %1, off sc1" : "=v"(pv) : "v"(fa) : "memory");
    issue_A(2); fin(IC<12>{}, IC<12>{}, IC<-1>{}); LBAR();
    issue_A(3); fin(IC<13>{}, IC<12>{}, IC<8>{});
    asm volatile("" : "+v"(pv) :: "memory");
    if (!__all(pv == tag)) spin_flags(fa, true, tag);
    STAMP(1);
    issue_B(0); issue_B(1); wait_vm<4>(); LBAR();
#pragma unroll
    for (int t = 0; t < 6; ++t) hv[t] = *(LAS const u32x4*)(smem + (t < 4 ? 65536 + t * 1024 : 45056 + t * 1024) + lw * 4096 + lane * 16);
    asm volatile("s_waitcnt lgkmcnt(0)" : "+v"(hv[0]), "+v"(hv[1]), "+v"(hv[2]), "+v"(hv[3]), "+v"(hv[4]), "+v"(hv[5]) :: "memory");
    issue_B(2); fin(IC<14>{}, IC<14>{}, IC<4>{}); LBAR();
    issue_B(3); issue_B(4); fin(IC<15>{}, IC<14>{}, IC<8>{}); LBAR();
    issue_A(5); issue_B(5); fin(IC<16>{}, IC<15>{}, IC<12>{}); LBAR();
    const gu64* fh = (const gu64*)(ws + WS_FH) + ((tm * 16 + ((lane >> 1) & 15)) * 4 + (ks >> 1) * 2 + (lane & 1));
    sfor<2, 12>([&](auto Jc) LAM {
      constexpr int j = decltype(Jc)::value;
      if constexpr (j == 9) asm volatile("global_load_dwordx2 %0, %1, off sc1" : "=v"(pv) : "v"(fh) : "memory");
      if constexpr (j & 3) issue_A(j + 4);
      issue_B(j + 4);
      fin(IC<15 + j>{}, IC<13 + j>{}, IC<((j & 3) == 0 || (j & 3) == 1 ? 12 : 16) + (j == 9 || j == 10)>{});
      if constexpr (j == 11) asm volatile("" : "+v"(pv) :: "memory");
      LBAR();
    });
    asm volatile("" :: "v"(tch));
    if (!__all(pv == tag)) spin_flags(fh, true, tag);
    STAMP(2);
    issue_T(0, 3); wait_vm<16>(); LBAR();
    issue_T(1, 3); wait_vm<16>(); LBAR();
    issue_T(2, 3); LBAR();
    issue_T(3, 3); wait_vm<24>(); LBAR();
    constexpr int DCUT[11] = {DSCHED};
    sfor<1, 11>([&](auto Ic) LAM {
      constexpr int i = decltype(Ic)::value;
      sfor<DCUT[i - 1], DCUT[i]>([&](auto Hc) LAM {
        constexpr int h = decltype(Hc)::value;
        if (h == 12 && lw == 3) {
          vL = OOB;
#pragma unroll
          for (int q = 0; q < 4; ++q) vD[q] = OOB;
        }
        issue_T(4 + h / 2, 2 - (h & 1));
      });
      static_assert(DCUT[i] <= (i == 1 ? 1 : 2 * i - 2) && DCUT[i] >= 2 * i - 6, "slot free / step issued");
      if (i == 10) STAMP(3);
      wait_vm<4 * (DCUT[i] - 2 * i + 6)>(); LBAR();
    });
    STAMP_OUT();
    if (b == 0 && wid == 7) {
#pragma unroll 1
      for (int q = 0; q < 4; ++q) spin_flags((const gu64*)(ws + WS_FA) + (q * 64 + lane) * 4, true, tag);
      if (lane == 0) __hip_atomic_store((gu64*)(ws + WS_E), tag, RLX_AGENT);
    }
  } else {
    const int w = wid;
    const char* wse = ws;
    asm volatile("" : "+s"(wse));
    const u64 epoch = *(const __attribute__((address_space(4))) u64*)(wse + WS_E);
    const u64 tag = epoch * 6364136223846793005ull + 1442695040888963407ull;
    auto rW = __builtin_amdgcn_make_buffer_rsrc((void*)ws, 0, WS_END, 0x00020000);
    const unsigned lds0 = (unsigned)(size_t)smem;
    auto rd128 = [&](unsigned a, int o) { return *(LAS const bf16x8*)((LAS char*)(size_t)a + o); };
    auto rdtr = [&](unsigned a, int o, int d) {
      bf16x4 lo = __builtin_amdgcn_ds_read_tr16_b64_v4bf16((LAS bf16x4*)((LAS char*)(size_t)a + o));
      bf16x4 hi = __builtin_amdgcn_ds_read_tr16_b64_v4bf16((LAS bf16x4*)((LAS char*)(size_t)a + o + d));
      return __builtin_shufflevector(lo, hi, 0, 1, 2, 3, 4, 5, 6, 7);
    };
    auto stage1 = [&](auto NBc) LAM {
      constexpr int NB = decltype(NBc)::value, NF = NB == 4 ? 9 : 10, D = 4;
      const int i16 = lane & 15, g = lane >> 4;
      unsigned aX[2], aH[2], aW[2];
#pragma unroll
      for (int s = 0; s < 2; ++s) {
        aH[s] = opq(lds0 + i16 * 128 + (((4 * s + g) ^ ((i16 >> 1) & 7)) * 16)); aX[s] = opq(aH[s] + w * 4096);
        aW[s] = opq(lds0 + (8 * g + (i16 >> 2)) * 192 + ((s ^ (g & 1)) * 32) + (lane & 3) * 8 + wslot(0));
      }
      const unsigned aG = opq(lds0 + g * 16 + 159744);
      bf16x8 xf[16][2], fr[8];
      f32x4 G[NB][2];
      WAIT_LGKM0();
      BARRIER();
      STAMP(1);
      {
        f32x4 ab[2][4], z[2];
        auto rdF = [&](auto Fc) LAM {
          constexpr int F = decltype(Fc)::value, kq = F / NF, j = kq >> 1, s = kq & 1, f = F % NF;
          if (j && !s && !f) {
            constexpr int dl = j % 3 ? SLOT : -2 * SLOT;
#pragma unroll
            for (int t = 0; t < 2; ++t) { aX[t] = opq(aX[t] + dl); aH[t] = opq(aH[t] + dl); aW[t] = opq(aW[t] + (wslot(j) - wslot(j - 1))); }
          }
          if (f == 0) { xf[kq][0] = rd128(aX[s], 20480); xf[kq][1] = rd128(aX[s], 22528); }
          if constexpr (f < 4) fr[F & 7] = rd128(aH[s], f * 2048);
          else if constexpr (f < 4 + NB) fr[F & 7] = rdtr(aW[f & 1], ((f - 4) >> 1) * 64 + s * 6144, 768);
          else fr[F & 7] = rd128(aG, kq * 64);
        };
        auto mm = [&](auto Fc, int mb) LAM {
          constexpr int F = decltype(Fc)::value, kq = F / NF, f = F % NF;
          if constexpr (f < 4) mfx<kq == 0>(ab[mb][f], xf[kq][mb], fr[F & 7]);
          else if constexpr (f < 4 + NB) mfx<kq == 0>(G[f - 4][mb], fr[F & 7], xf[kq][mb]);
          else mfx<kq == 0>(z[mb], fr[F & 7], xf[kq][mb]);
        };
        sfor<0, D>(rdF);
        SB();
        sfor<0, 16 * NF>([&](auto Fc) LAM {
          constexpr int F = decltype(Fc)::value;
          if constexpr (F + D < 16 * NF && ((F + D) % (2 * NF)) == 0) { BARRIER(); SB(); }
          mm(Fc, 0); SB();
          if constexpr (F + D < 16 * NF) rdF(IC<F + D>{});
          SB();
          mm(Fc, 1); SB();
        });
        WAIT_LGKM0(); BARRIER();
        settle();
#pragma unroll
        for (int c = 0; c < 4; ++c) { pin(ab[0][c]); pin(ab[1][c]); }
        if constexpr (NB == 4) { pin(z[0]); pin(z[1]); }
        STAMP(2);
        const unsigned sgn = n1 < 8 ? 0u : 0x80008000u;
        const int sA = WS_AB + (((n1 >> 2) * 64 + m1 * 4 + w) * 16 + (n1 & 3) * 4) * 1024;
#pragma unroll
        for (int c = 0; c < 4; ++c) {
          u32x4 v = {cvtpk(ab[0][c][0], ab[0][c][1]) ^ sgn, cvtpk(ab[0][c][2], ab[0][c][3]) ^ sgn,
                     cvtpk(ab[1][c][0], ab[1][c][1]) ^ sgn, cvtpk(ab[1][c][2], ab[1][c][3]) ^ sgn};
          __builtin_amdgcn_raw_buffer_store_b128(v, rW, lane * 16 + c * 1024, sA, 16);
        }
        if (NB == 4 && n1 == 3 && lane < 16) {
#pragma unroll
          for (int mb = 0; mb < 2; ++mb) {
            float gv = __builtin_amdgcn_rcpf(1.0f + __builtin_amdgcn_exp2f(-1.4426950409f * z[mb][0]));
            __hip_atomic_store((gu32*)(ws + WS_G) + m1 * 128 + w * 32 + mb * 16 + lane, __builtin_bit_cast(unsigned, gv), RLX_AGENT);
          }
        }
        SB();
      }
      STAMP(3);
      {
        gu64* const fa = (gu64*)(ws + WS_FA) + ((m1 * 16 + n1) * 4 + w);
        f32x4 U[NB][2];
        float t, r;
        auto rdF = [&](auto Fc) LAM {
          constexpr int F = decltype(Fc)::value, kq = F / NB, j = kq >> 1, s = kq & 1, f = F % NB;
          if ((j == 0 || j == 1 || j == 4) && !s && !f) {
            constexpr int dl = j == 0 ? 147456 - wslot(7) : -73728;
#pragma unroll
            for (int c = 0; c < 2; ++c) aW[c] = opq(aW[c] + dl);
          }
          if (kq >= 14 && f == 0) { xf[kq][0] = rd128(aX[s], 20480); xf[kq][1] = rd128(aX[s], 22528); }
          fr[F & 7] = rdtr(aW[f & 1], (j == 0 ? 0 : j < 4 ? j - 1 : j - 4) * 12288 + (f >> 1) * 64 + s * 6144, 768);
        };
        auto act = [&](auto Fc) LAM {
          constexpr int F = decltype(Fc)::value, v = F >> 2, ph = F & 3;
          if constexpr (ph == 0) asm volatile("v_fmaak_f32 %0, %1, %1, 0x41b2e92f\n\tv_mul_f32 %0, %1, %0" : "=&v"(t) : "v"(G[v >> 2][0][v & 3]));
          if constexpr (ph == 1) {
            asm volatile("v_mul_f32 %0, 0xbdd2d3e8, %0" : "+v"(t));
            if constexpr (v > 0) asm volatile("v_mul_f32 %0, %0, %1" : "+v"(G[(v - 1) >> 2][0][(v - 1) & 3]) : "v"(r));
          }
          if constexpr (ph == 2) asm volatile("v_exp_f32 %0, %0" : "+v"(t));
          if constexpr (ph == 3) asm volatile("v_add_f32 %1, 1.0, %1\n\tv_rcp_f32 %0, %1" : "=v"(r), "+v"(t));
        };
        sfor<0, D>(rdF);
        SB();
        sfor<0, 16 * NB>([&](auto Fc) LAM {
          constexpr int F = decltype(Fc)::value, f = F % NB;
          if (F + D == FA_ST * 2 * NB) { wait_vm<0>(); if (lane == 0) __hip_atomic_store(fa, tag, RLX_AGENT); SB(); }
          if constexpr (((F + D) % (2 * NB)) == 0 && ((0x2e >> ((F + D) / (2 * NB))) & 1)) { BARRIER(); SB(); }
          mfx<F / NB == 0>(U[f][0], fr[F & 7], xf[F / NB][0]); SB();
          if constexpr (F + D < 16 * NB) rdF(IC<F + D>{});
          SB();
          mfx<F / NB == 0>(U[f][1], fr[F & 7], xf[F / NB][1]); SB();
          act(Fc); SB();
        });
        WAIT_LGKM0(); BARRIER();
        settle();
        asm volatile("v_mul_f32 %0, %0, %1" : "+v"(G[NB - 1][0][3]) : "v"(r));
#pragma unroll
        for (int c = 0; c < NB; ++c) { pin(U[c][0]); pin(U[c][1]); }
        STAMP(4);
        LAS char* const hs = smem + w * 4096 + lane * 16;
#pragma unroll
        for (int i = 0; i < NB / 2; ++i) {
          const f32x4 h0 = G[2 * i][0] * U[2 * i][0], h1 = G[2 * i + 1][0] * U[2 * i + 1][0];
          u32x4 v = {cvtpk(h0[0], h0[1]), cvtpk(h0[2], h0[3]), cvtpk(h1[0], h1[1]), cvtpk(h1[2], h1[3])};
          *(LAS u32x4*)(hs + (i < 2 ? 65536 + i * 2048 : 49152)) = v;
        }
        SB();
#pragma unroll
        for (int i = 0; i < NB / 2; ++i) {
          const f32x4 g0 = G[2 * i][1], u0 = U[2 * i][1], g1 = G[2 * i + 1][1], u1 = U[2 * i + 1][1];
          u32x4 v = {cvtpk(geglu(g0[0], u0[0]), geglu(g0[1], u0[1])), cvtpk(geglu(g0[2], u0[2]), geglu(g0[3], u0[3])),
                     cvtpk(geglu(g1[0], u1[0]), geglu(g1[1], u1[1])), cvtpk(geglu(g1[2], u1[2]), geglu(g1[3], u1[3]))};
          *(LAS u32x4*)(hs + (i < 2 ? 66560 + i * 2048 : 50176)) = v;
        }
        WAIT_LGKM0();
        SB();
      }
    };
    if (big) stage1(IC<6>{}); else stage1(IC<4>{});
    STAMP(5);
    const int wm = w >> 1, wn = w & 1;
    const int lane = lane_id(), i16 = lane & 15, g = lane >> 4, q = i16 >> 2, p = lane & 3;
    float gv[2];
    {
      unsigned aA[2], aL[2], aZ[2];
#pragma unroll
      for (int s = 0; s < 2; ++s) { aL[s] = opq(lds0 + i16 * 128 + (((4 * s + g) ^ ((i16 >> 1) & 7)) * 16) + wm * 8192); aA[s] = opq(aL[s] + 98304); aZ[s] = opq(aL[s] + 114688); }
      const unsigned aB = opq(lds0 + lane * 16 + wn * 4096 + 32768);
      f32x4 acc[4][4] = {};
      bf16x8 fa[2][4], fb[2][4];
      auto rdA = [&](int set, int mb, int slot, int s) { fa[set][mb] = rd128(slot == 0 ? aZ[s] : slot == 3 ? aL[s] : aA[s], (slot == 1 ? 49152 : slot == 3 ? 16384 : 0) + mb * 2048); };
      auto rdB = [&](int set, int nb, int slot, int s) { fb[set][nb] = rd128(aB, ((slot + 3) & 3) * 16384 + s * 8192 + nb * 1024); };
      auto mm = [&](int set, int nb, int mb) { mfa(acc[nb][mb], fb[set][nb], fa[set][mb]); };
#pragma unroll
      for (int i = 0; i < 16; ++i) pin(acc[i >> 2][i & 3]);
      BARRIER();
      STAMP(6);
#pragma unroll
      for (int i = 0; i < 4; ++i) { rdA(0, i, 0, 0); rdB(0, i, 0, 0); }
      SB();
#pragma unroll 1
      for (int it = 0; it < 4; ++it) {
#pragma unroll
        for (int u = 0; u < 4; ++u) {
#pragma unroll
          for (int s = 0; s < 2; ++s) {
            const int cur = s, nxt = s ^ 1, ns = s ^ 1, nslot = s ? ((u + 1) & 3) : u;
            if (u == 2 && s == 0) { const unsigned z = it == 0 ? 131072 : it == 1 ? 0 : 114688; aZ[0] = opq(aL[0] + z); aZ[1] = opq(aL[1] + z); SB(); }
            if (s == 1) { WAIT_LGKM0(); BARRIER(); SB(); }
            mm(cur, 0, 0); SB(); rdA(nxt, 0, nslot, ns); SB();
            mm(cur, 1, 0); SB(); rdB(nxt, 0, nslot, ns); SB();
            mm(cur, 2, 0); SB(); rdB(nxt, 1, nslot, ns); SB();
            mm(cur, 3, 0); SB(); rdB(nxt, 2, nslot, ns); SB();
            mm(cur, 0, 1); SB(); rdB(nxt, 3, nslot, ns); SB();
            mm(cur, 1, 1); SB(); rdA(nxt, 1, nslot, ns); SB();
            mm(cur, 2, 1); SB(); rdA(nxt, 2, nslot, ns); SB();
            mm(cur, 3, 1); SB(); rdA(nxt, 3, nslot, ns); SB();
            mm(cur, 0, 2); SB(); mm(cur, 1, 2); SB(); mm(cur, 2, 2); SB(); mm(cur, 3, 2); SB();
            mm(cur, 0, 3); SB(); mm(cur, 1, 3); SB(); mm(cur, 2, 3); SB(); mm(cur, 3, 3); SB();
          }
        }
      }
      settle();
#pragma unroll
      for (int i = 0; i < 16; ++i) pin(acc[i >> 2][i & 3]);
      STAMP(7);
#pragma unroll
      for (int mb = 0; mb < 2; ++mb)
        gv[mb] = __builtin_bit_cast(float, __hip_atomic_load((gu32*)(ws + WS_G) + tm * 128 + (ks >> 1) * 64 + wm * 32 + mb * 16 + i16, RLX_AGENT));
      const int sY = WS_YP + wgl * 24576 + ((w - ks - 1) & 3) * 8192;
#pragma unroll
      for (int mb = 0; mb < 4; ++mb)
#pragma unroll
        for (int h = 0; h < 2; ++h) {
          u32x4 v = {cvtpk(acc[2 * h][mb][0], acc[2 * h][mb][1]), cvtpk(acc[2 * h][mb][2], acc[2 * h][mb][3]),
                     cvtpk(acc[2 * h + 1][mb][0], acc[2 * h + 1][mb][1]), cvtpk(acc[2 * h + 1][mb][2], acc[2 * h + 1][mb][3])};
          if (w != ks) __builtin_amdgcn_raw_buffer_store_b128(v, rW, lane * 16 + (mb * 2 + h) * 1024, sY, 16);
          else *(LAS u32x4*)(smem + 16384 + (mb * 2 + h) * 1024 + lane * 16) = v;
        }
      WAIT_LGKM0();
    }
    STAMP(8);
    {
      const unsigned aH0 = opq(lds0 + lane * 16 + wm * 2048), aH1 = opq(aH0 + 98304);
      const int fsw = (q >> 1) | ((g & 1) << 1);
      unsigned aD0[2], aD1[2];
#pragma unroll
      for (int c = 0; c < 2; ++c) { aD0[c] = opq(lds0 + (4 * g + q) * 128 + (((2 * wn + c) ^ fsw) * 32) + p * 8 + 32768); aD1[c] = opq(aD0[c] - 32768); }
      f32x4 acc[2][2] = {};
      u32x4 y[4][2];
      bf16x8 hf[4][2], df[4][2];
      gu64* const fy = (gu64*)(ws + WS_FY) + (wgl * 4 + w) * 8;
      const gu64* fz = (const gu64*)(ws + WS_FY) + ((((xcd & 4) | (lane & 3)) * 32 + idx) * 4 + ks) * 8;
      const bool fzl = (lane & 3) != ks;
      u64 pv; bool late = true;
      auto load_y = [&]() {
#pragma unroll
        for (int o = 0; o < 3; ++o) {
          const int sY = WS_YP + ((((xcd & 4) | ((ks - o - 1) & 3)) * 32 + idx) * 3 + o) * 8192 + (wm * 4 + wn) * 1024;
#pragma unroll
          for (int mb = 0; mb < 2; ++mb)
            asm volatile("buffer_load_dwordx4 %0, %1, %2, %3 offen offset:%4 sc1" : "=v"(y[o][mb]) : "v"(lane * 16), "s"(rW), "s"(sY), "n"(mb * 2048) : "memory");
        }
      };
      auto rh = [&](int sl, int j, int mb) { hf[j][mb] = rd128(sl < 4 ? aH1 : aH0, (sl == 0 ? 2 : sl == 2 ? 3 : sl == 3 ? 0 : 1) * 16384 + j * 4096 + mb * 1024); };
      auto rd = [&](int sl, int j, int c) { df[j][c] = rdtr(sl == 0 ? aD1[c] : aD0[c], (sl ? (sl + 2) & 3 : 0) * 16384 + j * 4096, 2048); };
      auto kstep = [&](int j, int sl, int jn) {
        mfa(acc[0][0], df[j][0], hf[j][0]); SB(); if (jn >= 0) rd(sl, jn, 0); SB();
        mfa(acc[1][0], df[j][1], hf[j][0]); SB(); if (jn >= 0) { rh(sl, jn, 0); rh(sl, jn, 1); } SB();
        mfa(acc[0][1], df[j][0], hf[j][1]); SB(); if (jn >= 0) rd(sl, jn, 1); SB();
        mfa(acc[1][1], df[j][1], hf[j][1]); SB();
      };
#pragma unroll
      for (int i = 0; i < 4; ++i) pin(acc[i >> 1][i & 1]);
      BARRIER();
#pragma unroll
      for (int j = 0; j < 2; ++j) { rd(0, j, 0); rh(0, j, 0); rd(0, j, 1); rh(0, j, 1); }
      y[3][0] = *(LAS const u32x4*)(smem + 16384 + (wm * 4 + wn) * 1024 + lane * 16);
      y[3][1] = *(LAS const u32x4*)(smem + 16384 + (wm * 4 + 2 + wn) * 1024 + lane * 16);
      asm volatile("s_waitcnt lgkmcnt(0)" : "+v"(y[3][0]), "+v"(y[3][1]) :: "memory");
      SB();
      sfor<0, 10>([&](auto Dc) LAM {
        constexpr int d = decltype(Dc)::value, sl = d % 5;
        if (d == FY_STEP && w != ks) { wait_vm<0>(); if (lane == 0) __hip_atomic_store(fy, tag, RLX_AGENT); SB(); }
        if constexpr (d == FY_POLL) { asm volatile("global_load_dwordx2 %0, %1, off sc1" : "=v"(pv) : "v"(fz) : "memory"); SB(); }
        if constexpr (d == FY_CHK) {
          asm volatile("s_waitcnt vmcnt(0)" : "+v"(pv) :: "memory");
          late = !__all(!fzl || pv == tag);
          if (!late) load_y();
          SB();
        }
        kstep(0, sl, 2); kstep(1, sl, 3);
        BARRIER(); SB();
        kstep(2, (sl + 1) % 5, 0); kstep(3, (sl + 1) % 5, 1);
      });
      kstep(0, 0, 2); kstep(1, 0, -1); kstep(2, 0, -1);
      settle();
#pragma unroll
      for (int i = 0; i < 4; ++i) pin(acc[i >> 1][i & 1]);
      STAMP(9);
      if (late) { spin_flags(fz, fzl, tag); load_y(); }
      asm volatile("s_waitcnt vmcnt(0)" : "+v"(y[0][0]), "+v"(y[0][1]), "+v"(y[1][0]), "+v"(y[1][1]), "+v"(y[2][0]), "+v"(y[2][1]) :: "memory");
      STAMP(10);
      auto rO = __builtin_amdgcn_make_buffer_rsrc((void*)out, 0, 2048 * 512 * 2, 0x00020000);
#pragma unroll
      for (int mb = 0; mb < 2; ++mb) {
        const int row = tm * 128 + (ks >> 1) * 64 + wm * 32 + 8 * q + 4 * mb + p;
        const float gg = gv[mb], gn = 1.0f - gg;
        unsigned pk[2][2];
#pragma unroll
        for (int nb = 0; nb < 2; ++nb)
#pragma unroll
          for (int e = 0; e < 2; ++e) {
            const float lo = (bflo(y[0][mb][nb * 2 + e]) + bflo(y[1][mb][nb * 2 + e])) + (bflo(y[2][mb][nb * 2 + e]) + bflo(y[3][mb][nb * 2 + e]));
            const float hi = (bfhi(y[0][mb][nb * 2 + e]) + bfhi(y[1][mb][nb * 2 + e])) + (bfhi(y[2][mb][nb * 2 + e]) + bfhi(y[3][mb][nb * 2 + e]));
            pk[nb][e] = cvtpk(gg * lo + gn * acc[nb][mb][2 * e], gg * hi + gn * acc[nb][mb][2 * e + 1]);
          }
        auto s0 = __builtin_amdgcn_permlane16_swap(pk[0][0], pk[1][0], false, false);
        auto s1 = __builtin_amdgcn_permlane16_swap(pk[0][1], pk[1][1], false, false);
        u32x4 v = {s0[0], s1[0], s0[1], s1[1]};
        __builtin_amdgcn_raw_buffer_store_b128(v, rO, (row * 512 + tn * 128 + (ks & 1) * 64 + wn * 32 + (g & 1) * 16 + (g >> 1) * 8) * 2, 0, AUX_O);
      }
      STAMP(11);
      STAMP_OUT();
    }
  }
#endif
}

extern "C" void kernel_launch(void* const* d_in, const int* in_sizes, int n_in, void* d_out, int out_size, void* d_ws,
                              size_t ws_size, hipStream_t stream) {
  (void)in_sizes; (void)n_in; (void)out_size; (void)ws_size;
  constexpr int kLds = 163840;
  (void)hipFuncSetAttribute((const void*)fused_kernel, hipFuncAttributeMaxDynamicSharedMemorySize, kLds);
  fused_kernel<<<256, 512, kLds, stream>>>((const u16*)d_in[0], (const u16*)d_in[1], (const u16*)d_in[2], (const u16*)d_in[3],
                                           (const u16*)d_in[4], (const u16*)d_in[6], (const u16*)d_in[7], (const u16*)d_in[8],
                                           (const u16*)d_in[9], (u16*)d_out, (char*)d_ws + WSOFF);
}
```

```cpp
#include <hip/hip_runtime.h>
#include <hip/hip_bf16.h>

typedef unsigned short u16;
typedef unsigned long long u64;
typedef __bf16 bf16x8 __attribute__((ext_vector_type(8)));
typedef __bf16 bf16x4 __attribute__((ext_vector_type(4)));
typedef __bf16 bf16x2 __attribute__((ext_vector_type(2)));
typedef float f32x4 __attribute__((ext_vector_type(4)));
typedef float f32x2 __attribute__((ext_vector_type(2)));
typedef unsigned u32x4 __attribute__((ext_vector_type(4)));
typedef unsigned u32x2 __attribute__((ext_vector_type(2)));
typedef __attribute__((address_space(1))) u64 gu64;
typedef __attribute__((address_space(1))) unsigned gu32;
#define LAS __attribute__((address_space(3)))
#define DI __device__ __forceinline__
#define SB() __builtin_amdgcn_sched_barrier(0)
#define BARRIER() do { asm volatile("" ::: "memory"); __builtin_amdgcn_s_barrier(); asm volatile("" ::: "memory"); } while (0)
#define WAIT_LGKM0() __builtin_amdgcn_s_waitcnt(0xC07F)
#define RLX_AGENT __ATOMIC_RELAXED, __HIP_MEMORY_SCOPE_AGENT
template <int V> struct IC { static constexpr int value = V; };
template <int I, int N, class Fn> DI void sfor(Fn&& fn) { if constexpr (I < N) { fn(IC<I>{}); sfor<I + 1, N>(fn); } }
#define LAM __attribute__((always_inline))
template <int N> DI void wait_vm() { asm volatile("s_waitcnt vmcnt(%0)" :: "n"(N) : "memory"); }

#define K1MODE 2
#define FA_KQ 4
#define PH 14
#define SH 1
#define SELH (tm & 3)
#define FY_STEP 3
#define FY_POLL 7
#define FY_SUM 10
#define FY_CHK 9
#define PA0 5
#define SA0 1
#define PA1 9
#define SA1 1
#define PA2 14
#define SA2 1
#define PA3 18
#define SA3 1
#define SPIN_SLEEP 1
#define SEL (tm & 3)
#define SEL0 SEL
#define SEL1 ((tm & 3) == 2 ? 3 : (tm & 3))
#define SEL2 SEL
#define SEL3 SEL
#define EB0 1
#define PT 0
#define TC0 12
#define TC1 -1
#define U0E 0
#define TWG 1
#define TWN 3
#define WSOFF 0
#define DSCHED 1, 3, 5, 7, 9, 10, 12, 14, 16, 18, 20, 22, 22, 22
#define YPOS(i) (-1)
#define NPOLL 2
#define HDIR 0
#define FH_IT 1
#define HORD 0
#define AUX_O 16
#define AUX_1 0

constexpr int WS_E = 0, WS_FA = 4096, WS_FH = 12288, WS_G = 20480, WS_FY = 65536;
constexpr int WS_AB = 1 << 20, WS_HB = 5 << 20, WS_YP = 11 << 20, WS_END = 17 << 20;
constexpr int SPIN_CAP = 1 << 20;

#define STAMP(i) do {} while (0)
#define STAMP_OUT() do {} while (0)

#define XS(i) do {} while (0)
#define LBAR() BARRIER()
#define FS(n) do {} while (0)

DI unsigned cvtpk(float lo, float hi) { f32x2 v = {lo, hi}; bf16x2 b = __builtin_convertvector(v, bf16x2); return __builtin_bit_cast(unsigned, b); }
DI float bflo(unsigned v) { return __builtin_bit_cast(float, v << 16); }
DI float bfhi(unsigned v) { return __builtin_bit_cast(float, v & 0xffff0000u); }
DI unsigned geglu(f32x2 x, f32x2 u, f32x2 c1, f32x2 c2) {
  f32x2 z = x * __builtin_elementwise_fma(x * x, c2, c1);
  f32x2 e = {__builtin_amdgcn_exp2f(z[0]), __builtin_amdgcn_exp2f(z[1])};
  e += 1.0f;
  f32x2 r = {__builtin_amdgcn_rcpf(e[0]), __builtin_amdgcn_rcpf(e[1])};
  r *= x * u;
  return cvtpk(r[0], r[1]);
}
DI void mfa(f32x4& c, bf16x8 a, bf16x8 b) { asm volatile("v_mfma_f32_16x16x32_bf16 %0, %1, %2, %0" : "+v"(c) : "v"(a), "v"(b)); }
DI void mf0(f32x4& c, bf16x8 a, bf16x8 b) { asm volatile("v_mfma_f32_16x16x32_bf16 %0, %1, %2, 0" : "=v"(c) : "v"(a), "v"(b)); }
template <bool FIRST> DI void mfx(f32x4& c, bf16x8 a, bf16x8 b) { if constexpr (FIRST) mf0(c, a, b); else mfa(c, a, b); }
DI void settle() { asm volatile("s_nop 7"); }
DI void sent() { SB(); asm volatile("s_nop 1"); SB(); }
DI void pin(f32x4& c) { asm volatile("" : "+v"(c)); }
DI unsigned opq(unsigned a) { asm volatile("" : "+v"(a)); return a; }
DI int lane_id() { int l; asm volatile("v_mbcnt_lo_u32_b32 %0, -1, 0\n\tv_mbcnt_hi_u32_b32 %0, -1, %0" : "=v"(l)); return l; }
DI u64 uni64(u64 v) {
  unsigned lo = __builtin_amdgcn_readfirstlane((unsigned)v), hi = __builtin_amdgcn_readfirstlane((unsigned)(v >> 32));
  return ((u64)hi << 32) | lo;
}
constexpr int SLOT = 36864;
constexpr int wslot(int j) { return (j & 3) == 0 ? 2 * SLOT + 8192 : (j & 3) == 1 ? 8192 : (j & 3) == 2 ? SLOT + 8192 : 131072; }
DI void spin_flags(const gu64* p, bool active, u64 tag) {
#pragma unroll 1
  for (int s = 0; s < SPIN_CAP; ++s) {
    u64 v = tag;
    if (active) v = __hip_atomic_load(p, RLX_AGENT);
    if (__all(v == tag)) break;
    __builtin_amdgcn_s_sleep(SPIN_SLEEP);
  }
}

__global__ __launch_bounds__(512, 2) void fused_kernel(
    const u16* __restrict__ X, const u16* __restrict__ Cr, const u16* __restrict__ Ci, const u16* __restrict__ Hr,
    const u16* __restrict__ Hi, const u16* __restrict__ Wg, const u16* __restrict__ Wu, const u16* __restrict__ Wd,
    const u16* __restrict__ GW, u16* __restrict__ out, char* __restrict__ ws) {
#if defined(__HIP_DEVICE_COMPILE__)
  extern __shared__ __attribute__((aligned(16))) char smem_[];
  LAS char* smem = (LAS char*)smem_;
  const int tid = threadIdx.x, lane = tid & 63;
  const int wid = __builtin_amdgcn_readfirstlane(tid >> 6);
  const int b = blockIdx.x, xcd = b & 7, idx = b >> 3;
  const int m1 = (~xcd & 4) * 2 + (idx & 7), n1 = ((xcd & 3) ^ 2) * 4 + (idx >> 3);
  const int blk0 = 22 * (n1 >> 2) + 6 * (n1 & 3) - (n1 == 15 ? 2 : 0);
  const bool big = ((n1 & 3) != 3) && (n1 != 14);
  const int ks = xcd & 3, tm = (xcd >> 2) * 8 + (idx >> 2), tn = idx & 3;
  const int wgl = xcd * 32 + idx;
  constexpr int OOB = 0x40000000;
  STAMP(0);
  asm volatile("" : "+s"(X), "+s"(Cr), "+s"(Ci), "+s"(Hr), "+s"(Hi), "+s"(Wg), "+s"(Wu), "+s"(Wd), "+s"(GW), "+s"(out), "+s"(ws));

  if (__builtin_expect(wid >= 4, 1)) {
    const int lw = wid - 4;
    auto rX = __builtin_amdgcn_make_buffer_rsrc((void*)X, 0, 2048 * 512 * 2, 0x00020000);
    auto rH = __builtin_amdgcn_make_buffer_rsrc((void*)(n1 < 8 ? Hr : Hi), 0, 512 * 512 * 2, 0x00020000);
    auto rG = __builtin_amdgcn_make_buffer_rsrc((void*)Wg, 0, 512 * 1376 * 2, 0x00020000);
    auto rU = __builtin_amdgcn_make_buffer_rsrc((void*)Wu, 0, 512 * 1376 * 2, 0x00020000);
    const int l3 = lane >> 3, l7 = lane & 7;
    constexpr int KM = (1 << K1MODE) - 1;
    const int k1 = K1MODE == 3 ? (m1 & 7) + 2 * (n1 & 3) : m1 + n1;
    auto kblk = [&](int j) { return (j & ~KM) | ((j + k1) & KM); };
    int vX[4], vH[2], vW[3];
#pragma unroll
    for (int i = 0; i < 4; ++i) {
      const int v = 32 * lw + 8 * i + l3;
      const int s = 32 * lw + 16 * (i & 1) + 8 * (lane >> 5) + 4 * (i >> 1) + (l3 & 3);
      vX[i] = (m1 * 128 + s) * 1024 + ((l7 ^ ((v >> 1) & 7)) * 16);
    }
    auto issue_x = [&](int j) {
#pragma unroll
      for (int i = 0; i < 4; ++i)
        __builtin_amdgcn_raw_ptr_buffer_load_lds(rX, smem + j % 3 * SLOT + 20480 + (lw * 4 + i) * 1024, 16, vX[i], kblk(j) * 128, 0, AUX_1);
    };
    FS(0);
    issue_x(0);
    FS(1);
#pragma unroll
    for (int i = 0; i < 2; ++i) {
      const int r = 16 * lw + 8 * i + l3;
      vH[i] = ((n1 & 7) * 64 + r) * 1024 + ((l7 ^ ((r >> 1) & 7)) * 16);
    }
#pragma unroll
    for (int i = 0; i < 3; ++i) {
      const int e = i * 64 + lane, rr = (e * 171) >> 11, s = e - 12 * rr, c = (((s >> 1) ^ (rr >> 3)) << 1) | (s & 1);
      vW[i] = (!big && c >= 8) ? OOB : (16 * lw + rr) * 2752 + blk0 * 32 + c * 16;
    }
    auto issue_w = [&](int j) {
      LAS char* R = smem + j % 3 * SLOT;
#pragma unroll
      for (int i = 0; i < 2; ++i) __builtin_amdgcn_raw_ptr_buffer_load_lds(rH, R + (lw * 2 + i) * 1024, 16, vH[i], kblk(j) * 128, 0, AUX_1);
#pragma unroll
      for (int i = 0; i < 3; ++i) __builtin_amdgcn_raw_ptr_buffer_load_lds(rG, smem + wslot(j) + (lw * 3 + i) * 1024, 16, vW[i], kblk(j) * (64 * 2752), 0, AUX_1);
    };
    auto issue_u = [&](int j) {
      LAS char* R = smem + (j == 0 ? 147456 : j < 4 ? 61440 + j * 12288 : (j - 4) * 12288);
#pragma unroll
      for (int i = 0; i < 3; ++i) __builtin_amdgcn_raw_ptr_buffer_load_lds(rU, R + (lw * 3 + i) * 1024, 16, vW[i], kblk(j) * (64 * 2752), 0, AUX_1);
    };
    if (!big && lw == 0) {
      auto rGW = __builtin_amdgcn_make_buffer_rsrc((void*)GW, 0, 1024, 0x00020000);
      __builtin_amdgcn_raw_ptr_buffer_load_lds(rGW, smem + 159744, 16, kblk(l3) * 128 + l7 * 16, 0, 0, 0);
    }
    FS(2);
    issue_w(0); issue_x(1);
    FS(3);
    auto rC = __builtin_amdgcn_make_buffer_rsrc((void*)(ks < 2 ? Cr : Ci), 0, 2048 * 2048 * 2, 0x00020000);
    auto rW = __builtin_amdgcn_make_buffer_rsrc((void*)ws, 0, WS_END, 0x00020000);
    auto rD = __builtin_amdgcn_make_buffer_rsrc((void*)Wd, 0, 1376 * 512 * 2, 0x00020000);
    int vC[4], vD[4];
#pragma unroll
    for (int i = 0; i < 4; ++i) {
      const int v = 32 * lw + 8 * i + l3;
      const int s = 32 * lw + 16 * (i & 1) + 8 * (lane >> 5) + 4 * (i >> 1) + (l3 & 3);
      vC[i] = (tm * 128 + (s ^ ((ks & 2) * 32))) * 4096 + (ks & 1) * 2048 + ((l7 ^ ((v >> 1) & 7)) * 16);
      const int r = 8 * i + l3;
      vD[i] = r * 1024 + (tn * 128 + (ks & 1) * 64) * 2 + (((l7 >> 1) ^ ((r >> 1) & 3)) * 32) + (l7 & 1) * 16;
    }
    int vL = lane * 16;
    const int sB = WS_AB + (((ks >> 1) * 2 + (tn >> 1)) * 64 + (ks & 1) * 32) * 16384 + ((lw >> 1) * 16 + (tn & 1) * 8 + ((lw ^ ks) & 1) * 4) * 1024;
    const int sHb = WS_HB + (tm * 2 + (ks >> 1)) * (43 * 4096);
    auto linear = [&](auto Nc, LAS char* R, int v, int so) LAM {
      sfor<0, decltype(Nc)::value>([&](auto Ic) LAM { __builtin_amdgcn_raw_ptr_buffer_load_lds(rW, R, 16, v, so, decltype(Ic)::value * 1024, 16); });
    };
    const int k2 = tn + 2 * ((tm >> 2) & 1), k3 = (11 * tn + ((43 * (tm & 7)) >> 3)) % 43 + lw;
    auto kk = [&](int st) { return (((st >> 2) + tm) & 3) * 4 + ((st + k2) & 3); };
    auto issue_A = [&](int st) {
      LAS char* R = smem + ((st & 3) == 1 ? 9 : (st & 3) == 2 ? 6 : (st & 3) == 3 ? 1 : st == 4 ? 8 : st == 8 ? 0 : 7) * 16384 + lw * 4096;
#pragma unroll
      for (int i = 0; i < 4; ++i) __builtin_amdgcn_raw_ptr_buffer_load_lds(rC, R + i * 1024, 16, vC[i], kk(st) * 128, 0, 0);
    };
    auto issue_B = [&](int st) { linear(IC<4>{}, smem + 32768 + ((st + 3) & 3) * 16384 + lw * 4096, vL, sB + kk(st) * 32768); };
    auto issue_T = [&](int d, int part) {
      const int sl = d % 5; int kq = k3 + 4 * d; kq -= kq >= 43 ? 43 : 0;
      if (part & 1) linear(IC<4>{}, smem + (sl == 0 ? 8 : sl == 1 ? 7 : sl == 2 ? 9 : sl == 3 ? 6 : 1) * 16384 + lw * 4096, vL, sHb + kq * 4096);
      LAS char* R = smem + (sl ? 32768 + ((sl + 2) & 3) * 16384 : 0) + lw * 4096;
#pragma unroll
      for (int i = 0; i < 4; ++i) if (part & 2) __builtin_amdgcn_raw_ptr_buffer_load_lds(rD, R + i * 1024, 16, vD[i], kq * 32768, 0, 0);
    };
    const int sel[4] = {SEL0, SEL1, SEL2, SEL3}, selh = SELH;
    u32x4 hv[6];
    u64 tag;
    unsigned tch = 0;
    constexpr int P[4] = {PA0, PA1, PA2, PA3}, S[4] = {SA0, SA1, SA2, SA3};
    auto leave = [&](auto Kc, auto Lc, auto Nc) LAM {
      constexpr int K = decltype(Kc)::value, L = decltype(Lc)::value, N = decltype(Nc)::value;
      int n = 0;
      if constexpr (!HDIR && K >= PH && L <= PH + 3 * SH) n += PH + SH * selh >= L && PH + SH * selh <= K;
      sfor<0, 4>([&](auto Gc) LAM {
        constexpr int G = decltype(Gc)::value;
        if constexpr (K >= P[G] && L <= P[G] + 4 * S[G]) n += P[G] + S[G] * sel[G] >= L && P[G] + S[G] * sel[G] <= K;
      });
      if (n == 0) wait_vm<N>(); else if (n == 1) wait_vm<N + 4>(); else if (n == 2) wait_vm<N + 8>(); else wait_vm<N + 12>();
    };
    auto fin = [&](auto Kc, auto Lc, auto Nc) LAM {
      constexpr int K = decltype(Kc)::value, L = decltype(Lc)::value, N = decltype(Nc)::value;
      if constexpr (!HDIR && K >= PH && K <= PH + 3 * SH) {
        if (K == PH + SH * selh) {
          const int sHp = WS_HB + ((m1 * 2 + (lw >> 1)) * 43 + (blk0 >> 1)) * 4096 + (lw & 1) * 2048;
#pragma unroll
          for (int t = 0; t < 6; ++t)
            if (t < 4 || big) __builtin_amdgcn_raw_buffer_store_b128(hv[t], rW, vL + (t & 1) * 1024, sHp + (t >> 1) * 4096, 16);
          sent();
        }
      }
      if constexpr (PT && K == PT)
        asm volatile("buffer_load_dword %0, %1, %2, 0 offen" : "+v"(tch) : "v"(TC1 < 0 && lane >= 32 ? OOB : (tm * 128 + lw * 32 + (lane & 31)) * 4096 + (ks & 1) * 2048 + (lane < 32 ? kk(TC0) : kk(TC1 < 0 ? 0 : TC1)) * 128), "s"(rC) : "memory");
      sfor<0, 4>([&](auto Gc) LAM {
        constexpr int G = decltype(Gc)::value;
        if constexpr (K >= P[G] && K <= P[G] + 4 * S[G]) {
          if (K == P[G] + S[G] * sel[G]) {
            constexpr int R = (42 + TWN) / TWN, r0 = (G - TWG) * R;
            if constexpr (G >= TWG && G < TWG + TWN)
              asm volatile("buffer_load_dword %0, %1, %2, 0 offen" : "+v"(tch) : "v"(lane < (r0 + R > 43 ? 43 - r0 : R) ? (idx * 43 + r0 + lane) * 1024 + lw * 256 + (ks & 1) * 128 : OOB), "s"(rD) : "memory");
            issue_A(4 * G);
          }
        }
      });
      if constexpr (N >= 0) leave(Kc, Lc, Nc);
      if constexpr (!HDIR && L - 1 >= PH && L - 1 <= PH + 3 * SH && K > 16) {
        if (L - 1 == PH + SH * selh && lane == 0) __hip_atomic_store((gu64*)(ws + WS_FH) + ((m1 * 16 + n1) * 4 + lw), tag, RLX_AGENT);
      }
    };
    wait_vm<4>(); LBAR();
    if (U0E) issue_u(0);
    issue_w(1);
    const char* wse = ws;
    asm volatile("" : "+s"(wse));
    u64 epoch = *(const __attribute__((address_space(4))) u64*)(wse + WS_E);
    sfor<0, 6>([&](auto Kc) LAM {
      constexpr int k = decltype(Kc)::value;
      issue_x(k + 2); issue_w(k + 2);
      if (k == 0) asm volatile("" : "+s"(epoch));
      fin(Kc, IC<k - 1>{}, IC<9>{}); LBAR();
    });
    if (!U0E) issue_u(0);
    issue_u(1); fin(IC<6>{}, IC<5>{}, IC<U0E ? 3 : 6>{}); LBAR();
    issue_u(2); issue_u(3); issue_u(4); fin(IC<7>{}, IC<6>{}, IC<12>{}); LBAR();
    tag = epoch * 6364136223846793005ull + 1442695040888963407ull;
    const gu64* fa = (const gu64*)(ws + WS_FA) + ((((ks & 1) * 8 + l3) * 16 + (ks >> 1) * 8 + tn * 2 + ((lane >> 2) & 1)) * 4 + (lane & 3));
    u64 pv;
    fin(IC<8>{}, IC<6>{}, IC<9>{}); LBAR();
    issue_u(5); issue_u(6); fin(IC<9>{}, IC<7>{}, IC<12>{}); LBAR();
    issue_u(7); fin(IC<10>{}, IC<7>{}, IC<9>{}); LBAR();
    issue_A(1); fin(IC<11>{}, IC<10>{}, IC<4>{}); LBAR();
    asm volatile("global_load_dwordx2 %0, %1, off sc1" : "=v"(pv) : "v"(fa) : "memory");
    const bool first = (l3 >> 1) == (tm & 3);
    issue_A(2);
    asm volatile("s_waitcnt vmcnt(4)" : "+v"(pv) :: "memory");
    bool early = __all(!first || pv == tag);
    if (NPOLL > 1 && !early) {
      asm volatile("global_load_dwordx2 %0, %1, off sc1\n\ts_waitcnt vmcnt(0)" : "=v"(pv) : "v"(fa) : "memory");
      early = __all(!first || pv == tag);
    }
    if (early) issue_B(0);
    fin(IC<12>{}, IC<12>{}, IC<-1>{}); LBAR();
    issue_A(3);
    if (!early) { spin_flags(fa, first, tag); issue_B(0); }
    STAMP(1);
    issue_B(1); fin(IC<13>{}, IC<13>{}, IC<-1>{});
    if (early) leave(IC<13>{}, IC<13>{}, IC<8>{}); else leave(IC<13>{}, IC<13>{}, IC<4>{});
    LBAR();
#pragma unroll
    for (int t = 0; t < 6; ++t) hv[t] = *(LAS const u32x4*)(smem + (t < 4 ? 65536 + t * 1024 : 45056 + t * 1024) + lw * 4096 + lane * 16);
    asm volatile("global_load_dwordx2 %0, %1, off sc1" : "=v"(pv) : "v"(fa) : "memory");
    asm volatile("s_waitcnt lgkmcnt(0)" : "+v"(hv[0]), "+v"(hv[1]), "+v"(hv[2]), "+v"(hv[3]), "+v"(hv[4]), "+v"(hv[5]) :: "memory");
    issue_B(2); fin(IC<14>{}, IC<13>{}, IC<5>{}); LBAR();
    issue_B(3); leave(IC<14>{}, IC<14>{}, IC<8>{});
    asm volatile("" : "+v"(pv) :: "memory");
    if (!__all(pv == tag)) spin_flags(fa, true, tag);
    issue_B(4); fin(IC<15>{}, IC<14>{}, IC<8>{}); LBAR();
    issue_A(5); issue_B(5); fin(IC<16>{}, IC<15>{}, IC<12>{}); LBAR();
    const gu64* fh = (const gu64*)(ws + WS_FH) + ((tm * 16 + ((lane >> 1) & 15)) * 4 + (ks >> 1) * 2 + (lane & 1));
    constexpr int DCUT[14] = {DSCHED};
    sfor<2, 12>([&](auto Jc) LAM {
      constexpr int j = decltype(Jc)::value;
      if constexpr (j == 9) asm volatile("global_load_dwordx2 %0, %1, off sc1" : "=v"(pv) : "v"(fh) : "memory");
      if constexpr (j & 3) issue_A(j + 4);
      issue_B(j + 4);
      if constexpr (j == 8 && DCUT[0]) issue_T(0, 2);
      fin(IC<15 + j>{}, IC<13 + j>{}, IC<((j & 3) == 0 || (j & 3) == 1 ? 12 : 16) + (j == 9 || j == 10) + (j == 8 || j == 9 ? 4 * DCUT[0] : 0)>{});
      if constexpr (j == 11) asm volatile("" : "+v"(pv) :: "memory");
      LBAR();
    });
    asm volatile("" :: "v"(tch));
    if (!__all(pv == tag)) spin_flags(fh, true, tag);
    STAMP(2);
    sfor<1, 14>([&](auto Ic) LAM {
      constexpr int i = decltype(Ic)::value, d = i - 3;
      sfor<DCUT[i - 1], DCUT[i]>([&](auto Hc) LAM {
        constexpr int h = decltype(Hc)::value;
        if (h == 20 && lw == 3) {
          vL = OOB;
#pragma unroll
          for (int q = 0; q < 4; ++q) vD[q] = OOB;
        }
        issue_T(h / 2, 2 - (h & 1));
      });
      static_assert(DCUT[0] <= 1 && DCUT[i] <= (d <= 1 ? 2 * i + 2 : 2 * d + 6) && DCUT[i] >= 2 * d + 2, "slot free / step issued");
      if (i == 13) STAMP(3);
      if constexpr (i < 3) wait_vm<4 * (DCUT[i] - DCUT[0]) + (i == 1 ? 8 : 0)>();
      if constexpr (d >= 0) wait_vm<4 * (DCUT[i] - 2 * d - 2)>();
      LBAR();
    });
    STAMP_OUT();
    if (b == 0 && wid == 7) {
#pragma unroll 1
      for (int q = 0; q < 4; ++q) spin_flags((const gu64*)(ws + WS_FA) + (q * 64 + lane) * 4, true, tag);
      if (lane == 0) __hip_atomic_store((gu64*)(ws + WS_E), tag, RLX_AGENT);
    }
  } else {
    const int w = wid;
    const char* wse = ws;
    asm volatile("" : "+s"(wse));
    const u64 epoch = *(const __attribute__((address_space(4))) u64*)(wse + WS_E);
    const u64 tag = epoch * 6364136223846793005ull + 1442695040888963407ull;
    auto rW = __builtin_amdgcn_make_buffer_rsrc((void*)ws, 0, WS_END, 0x00020000);
    const unsigned lds0 = (unsigned)(size_t)smem;
    auto rd128 = [&](unsigned a, int o) { return *(LAS const bf16x8*)((LAS char*)(size_t)a + o); };
    auto rdtr = [&](unsigned a, int o, int d) {
      bf16x4 lo = __builtin_amdgcn_ds_read_tr16_b64_v4bf16((LAS bf16x4*)((LAS char*)(size_t)a + o));
      bf16x4 hi = __builtin_amdgcn_ds_read_tr16_b64_v4bf16((LAS bf16x4*)((LAS char*)(size_t)a + o + d));
      return __builtin_shufflevector(lo, hi, 0, 1, 2, 3, 4, 5, 6, 7);
    };
    const int wm = w >> 1, wn = w & 1;
    unsigned aX[2], aD0[2];
    const unsigned vl = opq(lane * 16);
    aD0[0] = opq(lds0 + (lane >> 2) * 128 + (((2 * wn) ^ ((lane >> 3) & 3)) * 32) + (lane & 3) * 8 + 32768);
    int sY = WS_YP + wgl * 24576 + w * 2048;
    asm volatile("" : "+s"(sY));
    auto stage1 = [&](auto NBc) LAM {
      constexpr int NB = decltype(NBc)::value, NF = NB == 4 ? 9 : 10, D = 4;
      const int i16 = lane & 15, g = lane >> 4;
      unsigned aH[2], aW[2];
#pragma unroll
      for (int s = 0; s < 2; ++s) {
        aH[s] = opq(lds0 + i16 * 128 + (((4 * s + g) ^ ((i16 >> 1) & 7)) * 16)); aX[s] = opq(aH[s] + w * 4096);
        aW[s] = opq(lds0 + (8 * g + (i16 >> 2)) * 192 + ((s ^ (g & 1)) * 32) + (lane & 3) * 8 + wslot(0));
      }
      const unsigned aG = opq(lds0 + g * 16 + 159744);
      bf16x8 xf[16][2], fr[8];
      f32x4 G[NB][2];
      WAIT_LGKM0();
      BARRIER();
      STAMP(1);
      {
        f32x4 ab[2][4], z[2];
        auto rdF = [&](auto Fc) LAM {
          constexpr int F = decltype(Fc)::value, kq = F / NF, j = kq >> 1, s = kq & 1, f = F % NF;
          if (j && !s && !f) {
            constexpr int dl = j % 3 ? SLOT : -2 * SLOT;
#pragma unroll
            for (int t = 0; t < 2; ++t) { aX[t] = opq(aX[t] + dl); aH[t] = opq(aH[t] + dl); aW[t] = opq(aW[t] + (wslot(j) - wslot(j - 1))); }
          }
          if (f == 0) { xf[kq][0] = rd128(aX[s], 20480); xf[kq][1] = rd128(aX[s], 22528); }
          if constexpr (f < 4) fr[F & 7] = rd128(aH[s], f * 2048);
          else if constexpr (f < 4 + NB) fr[F & 7] = rdtr(aW[f & 1], ((f - 4) >> 1) * 64 + s * 6144, 768);
          else fr[F & 7] = rd128(aG, kq * 64);
        };
        auto mm = [&](auto Fc, int mb) LAM {
          constexpr int F = decltype(Fc)::value, kq = F / NF, f = F % NF;
          if constexpr (f < 4) mfx<kq == 0>(ab[mb][f], xf[kq][mb], fr[F & 7]);
          else if constexpr (f < 4 + NB) mfx<kq == 0>(G[f - 4][mb], fr[F & 7], xf[kq][mb]);
          else mfx<kq == 0>(z[mb], fr[F & 7], xf[kq][mb]);
        };
        sfor<0, D>(rdF);
        SB();
        sfor<0, 16 * NF>([&](auto Fc) LAM {
          constexpr int F = decltype(Fc)::value;
          if constexpr (F + D < 16 * NF && ((F + D) % (2 * NF)) == 0) { BARRIER(); SB(); }
          mm(Fc, 0); SB();
          if constexpr (F + D < 16 * NF) rdF(IC<F + D>{});
          SB();
          mm(Fc, 1); SB();
        });
        WAIT_LGKM0(); BARRIER();
        settle();
#pragma unroll
        for (int c = 0; c < 4; ++c) { pin(ab[0][c]); pin(ab[1][c]); }
        if constexpr (NB == 4) { pin(z[0]); pin(z[1]); }
        STAMP(2);
        const unsigned sgn = n1 < 8 ? 0u : 0x80008000u;
        const int sA = WS_AB + (((n1 >> 2) * 64 + m1 * 4 + w) * 16 + (n1 & 3) * 4) * 1024;
#pragma unroll
        for (int c = 0; c < 4; ++c) {
          u32x4 v = {cvtpk(ab[0][c][0], ab[0][c][1]) ^ sgn, cvtpk(ab[0][c][2], ab[0][c][3]) ^ sgn,
                     cvtpk(ab[1][c][0], ab[1][c][1]) ^ sgn, cvtpk(ab[1][c][2], ab[1][c][3]) ^ sgn};
          SB();
          __builtin_amdgcn_raw_buffer_store_b128(v, rW, vl + c * 1024, sA, 16);
          sent();
        }
        if (NB == 4 && n1 == 3 && lane < 16) {
#pragma unroll
          for (int mb = 0; mb < 2; ++mb) {
            float gv = __builtin_amdgcn_rcpf(1.0f + __builtin_amdgcn_exp2f(-1.4426950409f * z[mb][0]));
            __hip_atomic_store((gu32*)(ws + WS_G) + m1 * 128 + w * 32 + mb * 16 + lane, __builtin_bit_cast(unsigned, gv), RLX_AGENT);
          }
        }
        SB();
      }
      STAMP(3);
      {
        gu64* const fa = (gu64*)(ws + WS_FA) + ((m1 * 16 + n1) * 4 + w);
        f32x4 U[NB][2];
        float t, r;
        auto rdF = [&](auto Fc) LAM {
          constexpr int F = decltype(Fc)::value, kq = F / NB, j = kq >> 1, s = kq & 1, f = F % NB;
          if ((j == 0 || j == 1 || j == 4) && !s && !f) {
            constexpr int dl = j == 0 ? 147456 - wslot(7) : -73728;
#pragma unroll
            for (int c = 0; c < 2; ++c) aW[c] = opq(aW[c] + dl);
          }
          if (kq >= 14 && f == 0) { xf[kq][0] = rd128(aX[s], 20480); xf[kq][1] = rd128(aX[s], 22528); }
          fr[F & 7] = rdtr(aW[f & 1], (j == 0 ? 0 : j < 4 ? j - 1 : j - 4) * 12288 + (f >> 1) * 64 + s * 6144, 768);
        };
        auto act = [&](auto Fc) LAM {
          constexpr int F = decltype(Fc)::value, v = F >> 2, ph = F & 3;
          if constexpr (ph == 0) asm volatile("v_fmaak_f32 %0, %1, %1, 0x41b2e92f\n\tv_mul_f32 %0, %1, %0" : "=&v"(t) : "v"(G[v >> 2][0][v & 3]));
          if constexpr (ph == 1) {
            asm volatile("v_mul_f32 %0, 0xbdd2d3e8, %0" : "+v"(t));
            if constexpr (v > 0) asm volatile("v_mul_f32 %0, %0, %1" : "+v"(G[(v - 1) >> 2][0][(v - 1) & 3]) : "v"(r));
          }
          if constexpr (ph == 2) asm volatile("v_exp_f32 %0, %0" : "+v"(t));
          if constexpr (ph == 3) asm volatile("v_add_f32 %1, 1.0, %1\n\tv_rcp_f32 %0, %1" : "=v"(r), "+v"(t));
        };
        sfor<0, D>(rdF);
        SB();
        sfor<0, 16 * NB>([&](auto Fc) LAM {
          constexpr int F = decltype(Fc)::value, f = F % NB;
          if (F + D == FA_KQ * NB) { wait_vm<0>(); if (lane == 0) __hip_atomic_store(fa, tag, RLX_AGENT); SB(); }
          if constexpr (((F + D) % (2 * NB)) == 0 && ((0x2e >> ((F + D) / (2 * NB))) & 1)) { BARRIER(); SB(); }
          mfx<F / NB == 0>(U[f][0], fr[F & 7], xf[F / NB][0]); SB();
          if constexpr (F + D < 16 * NB) rdF(IC<F + D>{});
          SB();
          mfx<F / NB == 0>(U[f][1], fr[F & 7], xf[F / NB][1]); SB();
          act(Fc); SB();
        });
        WAIT_LGKM0(); BARRIER();
        settle();
        asm volatile("v_mul_f32 %0, %0, %1" : "+v"(G[NB - 1][0][3]) : "v"(r));
#pragma unroll
        for (int c = 0; c < NB; ++c) { pin(U[c][0]); pin(U[c][1]); }
        STAMP(4);
        XS(8);
        LAS char* const hs = (LAS char*)(size_t)opq(lds0 + w * 4096 + 49152 + vl);
        f32x2 c1 = {-2.302208198f, -2.302208198f}, c2 = {-0.1029432397f, -0.1029432397f};
        asm volatile("" : "+v"(c1), "+v"(c2));
        u32x4 hp[NB / 2][2];
        auto half0 = [&](int i) {
          const f32x4 h0 = G[2 * i][0] * U[2 * i][0], h1 = G[2 * i + 1][0] * U[2 * i + 1][0];
          hp[i][0] = u32x4{cvtpk(h0[0], h0[1]), cvtpk(h0[2], h0[3]), cvtpk(h1[0], h1[1]), cvtpk(h1[2], h1[3])};
          SB();
        };
        auto half1 = [&](int i) {
          const f32x4 g0 = G[2 * i][1], u0 = U[2 * i][1], g1 = G[2 * i + 1][1], u1 = U[2 * i + 1][1];
          hp[i][1] = u32x4{geglu(g0.xy, u0.xy, c1, c2), geglu(g0.zw, u0.zw, c1, c2), geglu(g1.xy, u1.xy, c1, c2), geglu(g1.zw, u1.zw, c1, c2)};
          SB();
        };
        const int sHp = WS_HB + ((m1 * 2 + (w >> 1)) * 43 + (blk0 >> 1)) * 4096 + (w & 1) * 2048;
        auto put = [&](int i, int mb) {
          if (HDIR) __builtin_amdgcn_raw_buffer_store_b128(hp[i][mb], rW, vl + mb * 1024, sHp + i * 4096, 16);
          else *(LAS u32x4*)(hs + (i < 2 ? 16384 + i * 2048 : 0) + mb * 1024) = hp[i][mb];
          if (HDIR) sent(); else SB();
        };
#pragma unroll
        for (int i = 0; i < NB / 2; ++i) {
          if (HORD == 0) { half0(i); put(i, 0); half1(i); put(i, 1); }
          if (HORD == 1) { half0(i); half1(i); }
          if (HORD == 2) { half1(i); put(i, 1); half0(i); if (i) put(i - 1, 0); }
          XS(9 + i);
        }
        if (HORD == 1) for (int i = 0; i < NB; ++i) put(i >> 1, i & 1);
        if (HORD == 2) put(NB / 2 - 1, 0);
        XS(13);
        WAIT_LGKM0();
        XS(14);
        SB();
      }
    };
    if (big) stage1(IC<6>{}); else stage1(IC<4>{});
    STAMP(5);
    unsigned aH0, aH1, aD1[2];
    bf16x8 hf[4][2], df[4][2];
    auto rh = [&](int sl, int j, int mb) { hf[j][mb] = rd128(sl < 4 ? aH1 : aH0, (sl == 0 ? 2 : sl == 2 ? 3 : sl == 3 ? 0 : 1) * 16384 + j * 4096 + mb * 1024); };
    auto rd = [&](int sl, int j, int c) { df[j][c] = rdtr(sl == 0 ? aD1[c] : aD0[c], (sl ? (sl + 2) & 3 : 0) * 16384 + j * 4096, 2048); };
    u32x4 yq[6];
    f32x4 own[2][2];
    {
      unsigned aA[2], aL[2], aZ[2];
#pragma unroll
      for (int s = 0; s < 2; ++s) aZ[s] = opq(aX[s] + (114688 - SLOT + (wm - w) * 4096));
      const unsigned aB = opq(vl + (lds0 + wn * 2048 + 32768));
      f32x4 acc[4][4];
      bf16x8 fa[2][4], fb[2][4];
      auto rdA = [&](int set, int mb, int slot, int s) { fa[set][mb] = rd128(slot == 0 ? aZ[s] : slot == 3 ? aL[s] : aA[s], (slot == 1 ? 49152 : slot == 3 ? 16384 : 0) + (mb >> 1) * 8192 + (mb & 1) * 2048); };
      auto rdB = [&](int set, int nb, int slot, int s) { fb[set][nb] = rd128(aB, ((slot + 3) & 3) * 16384 + s * 8192 + (nb >> 1) * 4096 + (nb & 1) * 1024); };
      auto half = [&](int u, int s) LAM {
        const int cur = s, nxt = s ^ 1, ns = s ^ 1, nslot = s ? ((u + 1) & 3) : u;
        auto mm = [&](int nb, int mb) LAM { mfa(acc[nb][mb], fb[cur][nb], fa[cur][mb]); SB(); };
        if (s == 1) { WAIT_LGKM0(); BARRIER(); SB(); }
        mm(0, 0); rdA(nxt, 0, nslot, ns); SB();
        mm(1, 0); rdB(nxt, 0, nslot, ns); SB();
        mm(2, 0); rdB(nxt, 1, nslot, ns); SB();
        mm(3, 0); rdB(nxt, 2, nslot, ns); SB();
        mm(0, 1); rdB(nxt, 3, nslot, ns); SB();
        mm(1, 1); rdA(nxt, 1, nslot, ns); SB();
        mm(2, 1); rdA(nxt, 2, nslot, ns); SB();
        mm(3, 1); rdA(nxt, 3, nslot, ns); SB();
        mm(0, 2); mm(1, 2); mm(2, 2); mm(3, 2);
        mm(0, 3); mm(1, 3); mm(2, 3); mm(3, 3);
      };
      XS(15);
      BARRIER();
      STAMP(6);
      rdA(0, 0, 0, 0);
#pragma unroll
      for (int i = 0; i < 4; ++i) rdB(0, i, 0, 0);
#pragma unroll
      for (int i = 1; i < 4; ++i) rdA(0, i, 0, 0);
      SB();
#pragma unroll
      for (int s = 0; s < 2; ++s) { aL[s] = opq(aZ[s] - 114688); aA[s] = opq(aZ[s] - 16384); }
      aH0 = opq(vl + (lds0 + wm * 2048)); aH1 = opq(aH0 + 98304);
      aD0[1] = opq(aD0[0] ^ 32); aD1[0] = opq(aD0[0] - 32768); aD1[1] = opq(aD0[1] - 32768);
#pragma unroll
      for (int i = 0; i < 16; ++i) { acc[i >> 2][i & 3] = f32x4{}; pin(acc[i >> 2][i & 3]); }
      SB();
#pragma unroll 1
      for (int it = 0; it < 4; ++it) {
        if (HDIR && it == FH_IT) { wait_vm<0>(); if (vl == 0) __hip_atomic_store((gu64*)(ws + WS_FH) + ((m1 * 16 + n1) * 4 + w), tag, RLX_AGENT); SB(); }
        half(0, 0); half(0, 1); half(1, 0); half(1, 1);
        { const unsigned z = it == 0 ? 131072 : it == 1 ? 0 : 114688; aZ[0] = opq(aL[0] + z); aZ[1] = opq(aL[1] + z); SB(); }
        half(2, 0); half(2, 1); half(3, 0); half(3, 1);
      }
      STAMP(7);
      XS(0);
#pragma unroll
      for (int j = 0; j < 2; ++j) { rd(0, j, 0); rh(0, j, 0); rd(0, j, 1); rh(0, j, 1); }
      SB();
      XS(1);
      settle();
#pragma unroll
      for (int i = 0; i < 16; ++i) pin(acc[i >> 2][i & 3]);
#pragma unroll
      for (int i = 0; i < 4; ++i) own[i >> 1][i & 1] = acc[i >> 1][i & 1];
#pragma unroll
      for (int i = 0; i < 6; ++i) {
        const int nb = ((i >> 1) + 1 & 1) * 2, mb = ((i >> 1) + 1 & 2) + (i & 1);
        yq[i] = u32x4{cvtpk(acc[nb][mb][0], acc[nb][mb][1]), cvtpk(acc[nb][mb][2], acc[nb][mb][3]),
                      cvtpk(acc[nb + 1][mb][0], acc[nb + 1][mb][1]), cvtpk(acc[nb + 1][mb][2], acc[nb + 1][mb][3])};
      }
      SB();
      XS(2);
#pragma unroll
      for (int i = 0; i < 6; ++i) if (YPOS(i) < 0) __builtin_amdgcn_raw_buffer_store_b128(yq[i], rW, vl + (i & 1) * 1024, sY + (i >> 1) * 8192, 16);
      sent();
    }
    STAMP(8);
    XS(3);
    {
      const int lane = vl >> 4, i16 = lane & 15, g = lane >> 4, q = i16 >> 2, p = lane & 3;
      f32x4 acc[2][2];
      u32x4 y[3][2];
      float gv[2], gn[2];
      gu64* const fy = (gu64*)(ws + WS_FY) + (wgl * 4 + w) * 8;
      const gu64* fz = (const gu64*)(ws + WS_FY) + ((((xcd & 4) | (lane & 3)) * 32 + idx) * 4 + w) * 8;
      const bool fzl = (lane & 3) != ks;
      u64 pv; bool late = true;
      int vO;
      u32x4 pick[2];
      auto rO = __builtin_amdgcn_make_buffer_rsrc((void*)out, 0, 2048 * 512 * 2, 0x00020000);
      asm volatile("" : "+s"(rO));
      auto load_y = [&]() {
#pragma unroll
        for (int o = 0; o < 3; ++o) {
          const int sY = WS_YP + (((xcd ^ (o + 1)) * 32 + idx) * 3 + o) * 8192 + w * 2048;
#pragma unroll
          for (int mb = 0; mb < 2; ++mb)
            asm volatile("buffer_load_dwordx4 %0, %1, %2, %3 offen offset:%4 sc1" : "=v"(y[o][mb]) : "v"(vl), "s"(rW), "s"(sY), "n"(mb * 1024) : "memory");
        }
      };
      auto kstep = [&](auto Fc, int j, int sl, int jn) LAM {
        constexpr bool F = decltype(Fc)::value;
        mfx<F>(acc[0][0], df[j][0], hf[j][0]); SB(); if (jn >= 0) rd(sl, jn, 0); SB();
        mfx<F>(acc[1][0], df[j][1], hf[j][0]); SB(); if (jn >= 0) { rh(sl, jn, 0); rh(sl, jn, 1); } SB();
        mfx<F>(acc[0][1], df[j][0], hf[j][1]); SB(); if (jn >= 0) rd(sl, jn, 1); SB();
        mfx<F>(acc[1][1], df[j][1], hf[j][1]); SB();
      };
      auto put = [&](auto Pc) LAM {
        sfor<0, 6>([&](auto Ic) LAM {
          constexpr int i = decltype(Ic)::value;
          if constexpr (YPOS(i) == decltype(Pc)::value) { __builtin_amdgcn_raw_buffer_store_b128(yq[i], rW, vl + (i & 1) * 1024, sY + (i >> 1) * 8192, 16); sent(); }
        });
      };
      auto sum_y = [&]() LAM {
        asm volatile("s_waitcnt vmcnt(0)" : "+v"(y[0][0]), "+v"(y[0][1]), "+v"(y[1][0]), "+v"(y[1][1]), "+v"(y[2][0]), "+v"(y[2][1]) :: "memory");
#pragma unroll
        for (int i = 0; i < 12; ++i) mfa(own[i & 1][(i >> 1) & 1], __builtin_bit_cast(bf16x8, pick[i & 1]), __builtin_bit_cast(bf16x8, y[i >> 2][(i >> 1) & 1]));
        settle();
#pragma unroll
        for (int i = 0; i < 4; ++i) { pin(own[i & 1][i >> 1]); own[i & 1][i >> 1] *= gv[i >> 1]; pin(own[i & 1][i >> 1]); }
        SB();
      };
      static_assert(YPOS(5) < 4 * FY_STEP, "stores before their flag");
      sfor<0, 10>([&](auto Dc) LAM {
        constexpr int d = decltype(Dc)::value, sl = d % 5;
        if (d == FY_STEP) { wait_vm<0>(); if (lane == 0) __hip_atomic_store(fy, tag, RLX_AGENT); SB(); }
        if constexpr (d == FY_POLL) {
          asm volatile("global_load_dwordx2 %0, %1, off sc1" : "=v"(pv) : "v"(fz) : "memory");
          asm volatile("buffer_load_dword %0, %2, %3, %4 offen sc1\n\tbuffer_load_dword %1, %2, %3, %4 offen offset:64 sc1"
                       : "=&v"(gv[0]), "=&v"(gv[1]) : "v"(i16 * 4), "s"(rW), "s"(WS_G + (tm * 128 + (ks >> 1) * 64 + wm * 32) * 4) : "memory");
          SB();
        }
        if constexpr (d == FY_CHK) {
          asm volatile("s_waitcnt vmcnt(0)" : "+v"(pv), "+v"(gv[0]), "+v"(gv[1]) :: "memory");
          late = !__all(!fzl || pv == tag);
          if (!late) load_y();
          gn[0] = 1.0f - gv[0]; gn[1] = 1.0f - gv[1];
          asm volatile("" : "+v"(gn[0]), "+v"(gn[1]));
          SB();
        }
        kstep(IC<d == 0>{}, 0, sl, 2); put(IC<4 * d>{}); kstep(IC<0>{}, 1, sl, 3); put(IC<4 * d + 1>{});
        if constexpr (d == 4) {
          const unsigned one = g == q ? 0x3f80u << (16 * (p & 1)) : 0u, o0 = p < 2 ? one : 0u, o1 = one - o0;
          pick[0] = u32x4{o0, o1, 0u, 0u}; pick[1] = u32x4{0u, 0u, o0, o1};
          vO = ((tm * 128 + (ks >> 1) * 64 + wm * 32 + 8 * q + p) * 512 + tn * 128 + (ks & 1) * 64 + wn * 32 + (g & 1) * 16 + (g >> 1) * 8) * 2;
          asm volatile("" : "+v"(pick[0]), "+v"(pick[1]), "+v"(vO));
          SB();
        }
        if constexpr (d == FY_SUM) { if (!late) sum_y(); }
        BARRIER(); SB();
        kstep(IC<0>{}, 2, (sl + 1) % 5, 0); put(IC<4 * d + 2>{}); kstep(IC<0>{}, 3, (sl + 1) % 5, 1); put(IC<4 * d + 3>{});
      });
      kstep(IC<0>{}, 0, 0, 2); kstep(IC<0>{}, 1, 0, -1); kstep(IC<0>{}, 2, 0, -1);
      settle();
#pragma unroll
      for (int i = 0; i < 4; ++i) pin(acc[i >> 1][i & 1]);
      STAMP(9);
      if (late || FY_SUM > 9) { if (late) { spin_flags(fz, fzl, tag); load_y(); } sum_y(); }
      STAMP(10);
#pragma unroll
      for (int mb = 0; mb < 2; ++mb) {
        const f32x4 r0 = gn[mb] * acc[0][mb] + own[0][mb], r1 = gn[mb] * acc[1][mb] + own[1][mb];
        auto s0 = __builtin_amdgcn_permlane16_swap(cvtpk(r0[0], r0[1]), cvtpk(r1[0], r1[1]), false, false);
        auto s1 = __builtin_amdgcn_permlane16_swap(cvtpk(r0[2], r0[3]), cvtpk(r1[2], r1[3]), false, false);
        u32x4 v = {s0[0], s1[0], s0[1], s1[1]};
        SB();
        __builtin_amdgcn_raw_buffer_store_b128(v, rO, vO, mb * 4096, AUX_O);
        sent();
      }
      STAMP(11);
      STAMP_OUT();
    }
  }
#endif
}

extern "C" void kernel_launch(void* const* d_in, const int* in_sizes, int n_in, void* d_out, int out_size, void* d_ws,
                              size_t ws_size, hipStream_t stream) {
  (void)in_sizes; (void)n_in; (void)out_size; (void)ws_size;
  constexpr int kLds = 163840;
  (void)hipFuncSetAttribute((const void*)fused_kernel, hipFuncAttributeMaxDynamicSharedMemorySize, kLds);
  fused_kernel<<<256, 512, kLds, stream>>>((const u16*)d_in[0], (const u16*)d_in[1], (const u16*)d_in[2], (const u16*)d_in[3],
                                           (const u16*)d_in[4], (const u16*)d_in[6], (const u16*)d_in[7], (const u16*)d_in[8],
                                           (const u16*)d_in[9], (u16*)d_out, (char*)d_ws + WSOFF);
}
```

```cpp
#include <hip/hip_runtime.h>
#include <hip/hip_bf16.h>

typedef unsigned short u16;
typedef unsigned long long u64;
typedef __bf16 bf16x8 __attribute__((ext_vector_type(8)));
typedef __bf16 bf16x4 __attribute__((ext_vector_type(4)));
typedef __bf16 bf16x2 __attribute__((ext_vector_type(2)));
typedef float f32x4 __attribute__((ext_vector_type(4)));
typedef float f32x2 __attribute__((ext_vector_type(2)));
typedef unsigned u32x4 __attribute__((ext_vector_type(4)));
typedef unsigned u32x2 __attribute__((ext_vector_type(2)));
typedef __attribute__((address_space(1))) u64 gu64;
typedef __attribute__((address_space(1))) unsigned gu32;
#define LAS __attribute__((address_space(3)))
#define DI __device__ __forceinline__
#define SB() __builtin_amdgcn_sched_barrier(0)
#define BARRIER() do { asm volatile("" ::: "memory"); __builtin_amdgcn_s_barrier(); asm volatile("" ::: "memory"); } while (0)
#define WAIT_LGKM0() __builtin_amdgcn_s_waitcnt(0xC07F)
#define RLX_AGENT __ATOMIC_RELAXED, __HIP_MEMORY_SCOPE_AGENT
template <int V> struct IC { static constexpr int value = V; };
template <int I, int N, class Fn> DI void sfor(Fn&& fn) { if constexpr (I < N) { fn(IC<I>{}); sfor<I + 1, N>(fn); } }
#define LAM __attribute__((always_inline))
template <int N> DI void wait_vm() { asm volatile("s_waitcnt vmcnt(%0)" :: "n"(N) : "memory"); }

#define K1MODE 2
#define FA_KQ 4
#define PH 14
#define SH 1
#define SELH (tm & 3)
#define FYK 9
#define FYP 15
#define FYC 23
#define PSPIN 0
#define PA0 5
#define SA0 1
#define PA1 9
#define SA1 1
#define PA2 14
#define SA2 1
#define PA3 18
#define SA3 1
#define SPIN_SLEEP 1
#define SEL (tm & 3)
#define SEL0 SEL
#define SEL1 ((tm & 3) == 2 ? 3 : (tm & 3))
#define SEL2 SEL
#define SEL3 SEL
#define EB0 1
#define PT 0
#define TC0 12
#define TC1 -1
#define U0E 0
#define TWG 1
#define TWN 3
#define WSOFF 0
#define DSCHED 2, 5, 7, 9, 11, 13, 13, 15, 17, 17, 17
#define YPOS(i) (-1)
#define HFIRST 99
#define OWNP 6
#define GLATE 0
#define NPOLL 2
#define HDIR 0
#define FH_IT 1
#define HORD 0
#define AUX_O 16
#define AUX_1 0

constexpr int WS_E = 0, WS_FA = 4096, WS_FH = 12288, WS_G = 20480, WS_FY = 65536;
constexpr int WS_AB = 1 << 20, WS_HB = 5 << 20, WS_YP = 11 << 20, WS_END = 17 << 20;
constexpr int SPIN_CAP = 1 << 20;
constexpr int ND = 27;

#define STAMP(i) do {} while (0)
#define STAMP_OUT() do {} while (0)

#define XS(i) do {} while (0)
#define LBAR() BARRIER()
#define FS(n) do {} while (0)

DI unsigned cvtpk(float lo, float hi) { f32x2 v = {lo, hi}; bf16x2 b = __builtin_convertvector(v, bf16x2); return __builtin_bit_cast(unsigned, b); }
DI float bflo(unsigned v) { return __builtin_bit_cast(float, v << 16); }
DI float bfhi(unsigned v) { return __builtin_bit_cast(float, v & 0xffff0000u); }
DI unsigned geglu(f32x2 x, f32x2 u, f32x2 c1, f32x2 c2) {
  f32x2 z = x * __builtin_elementwise_fma(x * x, c2, c1);
  f32x2 e = {__builtin_amdgcn_exp2f(z[0]), __builtin_amdgcn_exp2f(z[1])};
  e += 1.0f;
  f32x2 r = {__builtin_amdgcn_rcpf(e[0]), __builtin_amdgcn_rcpf(e[1])};
  r *= x * u;
  return cvtpk(r[0], r[1]);
}
DI void mfa(f32x4& c, bf16x8 a, bf16x8 b) { asm volatile("v_mfma_f32_16x16x32_bf16 %0, %1, %2, %0" : "+v"(c) : "v"(a), "v"(b)); }
DI void mf0(f32x4& c, bf16x8 a, bf16x8 b) { asm volatile("v_mfma_f32_16x16x32_bf16 %0, %1, %2, 0" : "=v"(c) : "v"(a), "v"(b)); }
template <bool FIRST> DI void mfx(f32x4& c, bf16x8 a, bf16x8 b) { if constexpr (FIRST) mf0(c, a, b); else mfa(c, a, b); }
DI void settle() { asm volatile("s_nop 7"); }
DI void sent() { SB(); asm volatile("s_nop 1"); SB(); }
DI void pin(f32x4& c) { asm volatile("" : "+v"(c)); }
DI unsigned opq(unsigned a) { asm volatile("" : "+v"(a)); return a; }
DI int lane_id() { int l; asm volatile("v_mbcnt_lo_u32_b32 %0, -1, 0\n\tv_mbcnt_hi_u32_b32 %0, -1, %0" : "=v"(l)); return l; }
DI u64 uni64(u64 v) {
  unsigned lo = __builtin_amdgcn_readfirstlane((unsigned)v), hi = __builtin_amdgcn_readfirstlane((unsigned)(v >> 32));
  return ((u64)hi << 32) | lo;
}
constexpr int SLOT = 36864;
constexpr int wslot(int j) { return (j & 3) == 0 ? 2 * SLOT + 8192 : (j & 3) == 1 ? 8192 : (j & 3) == 2 ? SLOT + 8192 : 131072; }
DI void spin_flags(const gu64* p, bool active, u64 tag) {
#pragma unroll 1
  for (int s = 0; s < SPIN_CAP; ++s) {
    u64 v = tag;
    if (active) v = __hip_atomic_load(p, RLX_AGENT);
    if (__all(v == tag)) break;
    __builtin_amdgcn_s_sleep(SPIN_SLEEP);
  }
}

__global__ __launch_bounds__(512, 2) void fused_kernel(
    const u16* __restrict__ X, const u16* __restrict__ Cr, const u16* __restrict__ Ci, const u16* __restrict__ Hr,
    const u16* __restrict__ Hi, const u16* __restrict__ Wg, const u16* __restrict__ Wu, const u16* __restrict__ Wd,
    const u16* __restrict__ GW, u16* __restrict__ out, char* __restrict__ ws) {
#if defined(__HIP_DEVICE_COMPILE__)
  extern __shared__ __attribute__((aligned(16))) char smem_[];
  LAS char* smem = (LAS char*)smem_;
  const int tid = threadIdx.x, lane = tid & 63;
  const int wid = __builtin_amdgcn_readfirstlane(tid >> 6);
  const int b = blockIdx.x, xcd = b & 7, idx = b >> 3;
  const int m1 = (~xcd & 4) * 2 + (idx & 7), n1 = ((xcd & 3) ^ 2) * 4 + (idx >> 3);
  const int blk0 = 22 * (n1 >> 2) + 6 * (n1 & 3) - (n1 == 15 ? 2 : 0);
  const bool big = ((n1 & 3) != 3) && (n1 != 14);
  const int ks = xcd & 3, tm = (xcd >> 2) * 8 + (idx >> 2), tn = idx & 3;
  const int wgl = xcd * 32 + idx;
  constexpr int OOB = 0x40000000;
  STAMP(0);
  asm volatile("" : "+s"(X), "+s"(Cr), "+s"(Ci), "+s"(Hr), "+s"(Hi), "+s"(Wg), "+s"(Wu), "+s"(Wd), "+s"(GW), "+s"(out), "+s"(ws));

  if (__builtin_expect(wid >= 4, 1)) {
    const int lw = wid - 4;
    auto rX = __builtin_amdgcn_make_buffer_rsrc((void*)X, 0, 2048 * 512 * 2, 0x00020000);
    auto rH = __builtin_amdgcn_make_buffer_rsrc((void*)(n1 < 8 ? Hr : Hi), 0, 512 * 512 * 2, 0x00020000);
    auto rG = __builtin_amdgcn_make_buffer_rsrc((void*)Wg, 0, 512 * 1376 * 2, 0x00020000);
    auto rU = __builtin_amdgcn_make_buffer_rsrc((void*)Wu, 0, 512 * 1376 * 2, 0x00020000);
    const int l3 = lane >> 3, l7 = lane & 7;
    constexpr int KM = (1 << K1MODE) - 1;
    const int k1 = K1MODE == 3 ? (m1 & 7) + 2 * (n1 & 3) : m1 + n1;
    auto kblk = [&](int j) { return (j & ~KM) | ((j + k1) & KM); };
    int vX[4], vH[2], vW[3];
#pragma unroll
    for (int i = 0; i < 4; ++i) {
      const int v = 32 * lw + 8 * i + l3;
      const int s = 32 * lw + 16 * (i & 1) + 8 * (lane >> 5) + 4 * (i >> 1) + (l3 & 3);
      vX[i] = (m1 * 128 + s) * 1024 + ((l7 ^ ((v >> 1) & 7)) * 16);
    }
    auto issue_x = [&](int j) {
#pragma unroll
      for (int i = 0; i < 4; ++i)
        __builtin_amdgcn_raw_ptr_buffer_load_lds(rX, smem + j % 3 * SLOT + 20480 + (lw * 4 + i) * 1024, 16, vX[i], kblk(j) * 128, 0, AUX_1);
    };
    FS(0);
    issue_x(0);
    FS(1);
#pragma unroll
    for (int i = 0; i < 2; ++i) {
      const int r = 16 * lw + 8 * i + l3;
      vH[i] = ((n1 & 7) * 64 + r) * 1024 + ((l7 ^ ((r >> 1) & 7)) * 16);
    }
#pragma unroll
    for (int i = 0; i < 3; ++i) {
      const int e = i * 64 + lane, rr = (e * 171) >> 11, s = e - 12 * rr, c = (((s >> 1) ^ (rr >> 3)) << 1) | (s & 1);
      vW[i] = (!big && c >= 8) ? OOB : (16 * lw + rr) * 2752 + blk0 * 32 + c * 16;
    }
    auto issue_w = [&](int j) {
      LAS char* R = smem + j % 3 * SLOT;
#pragma unroll
      for (int i = 0; i < 2; ++i) __builtin_amdgcn_raw_ptr_buffer_load_lds(rH, R + (lw * 2 + i) * 1024, 16, vH[i], kblk(j) * 128, 0, AUX_1);
#pragma unroll
      for (int i = 0; i < 3; ++i) __builtin_amdgcn_raw_ptr_buffer_load_lds(rG, smem + wslot(j) + (lw * 3 + i) * 1024, 16, vW[i], kblk(j) * (64 * 2752), 0, AUX_1);
    };
    auto issue_u = [&](int j) {
      LAS char* R = smem + (j == 0 ? 147456 : j < 4 ? 61440 + j * 12288 : (j - 4) * 12288);
#pragma unroll
      for (int i = 0; i < 3; ++i) __builtin_amdgcn_raw_ptr_buffer_load_lds(rU, R + (lw * 3 + i) * 1024, 16, vW[i], kblk(j) * (64 * 2752), 0, AUX_1);
    };
    if (!big && lw == 0) {
      auto rGW = __builtin_amdgcn_make_buffer_rsrc((void*)GW, 0, 1024, 0x00020000);
      __builtin_amdgcn_raw_ptr_buffer_load_lds(rGW, smem + 159744, 16, kblk(l3) * 128 + l7 * 16, 0, 0, 0);
    }
    FS(2);
    issue_w(0); issue_x(1);
    FS(3);
    auto rC = __builtin_amdgcn_make_buffer_rsrc((void*)(ks < 2 ? Cr : Ci), 0, 2048 * 2048 * 2, 0x00020000);
    auto rW = __builtin_amdgcn_make_buffer_rsrc((void*)ws, 0, WS_END, 0x00020000);
    auto rD = __builtin_amdgcn_make_buffer_rsrc((void*)Wd, 0, 1376 * 512 * 2, 0x00020000);
    int vC[4], vD[4];
#pragma unroll
    for (int i = 0; i < 4; ++i) {
      const int v = 32 * lw + 8 * i + l3;
      const int s = 32 * lw + 16 * (i & 1) + 8 * (lane >> 5) + 4 * (i >> 1) + (l3 & 3);
      vC[i] = (tm * 128 + (s ^ ((ks & 2) * 32))) * 4096 + (ks & 1) * 2048 + ((l7 ^ ((v >> 1) & 7)) * 16);
      const int r = 8 * i + l3;
      vD[i] = r * 1024 + (tn * 128 + (ks & 1) * 64) * 2 + (((l7 >> 1) ^ ((r >> 1) & 3)) * 32) + (l7 & 1) * 16;
    }
    int vL = lane * 16;
    const int sB = WS_AB + (((ks >> 1) * 2 + (tn >> 1)) * 64 + (ks & 1) * 32) * 16384 + ((lw >> 1) * 16 + (tn & 1) * 8 + ((lw ^ ks) & 1) * 4) * 1024;
    const int sHb = WS_HB + (tm * 2 + (ks >> 1)) * (43 * 4096);
    auto linear = [&](auto Nc, LAS char* R, int v, int so) LAM {
      sfor<0, decltype(Nc)::value>([&](auto Ic) LAM { __builtin_amdgcn_raw_ptr_buffer_load_lds(rW, R, 16, v, so, decltype(Ic)::value * 1024, 16); });
    };
    const int k2 = tn + 2 * ((tm >> 2) & 1), k3 = (7 * tn + ((ND * (tm & 7)) >> 3)) % ND + lw;
    auto kk = [&](int st) { return (((st >> 2) + tm) & 3) * 4 + ((st + k2) & 3); };
    auto issue_A = [&](int st) {
      LAS char* R = smem + ((st & 3) == 1 ? 9 : (st & 3) == 2 ? 6 : (st & 3) == 3 ? 1 : st == 4 ? 8 : st == 8 ? 0 : 7) * 16384 + lw * 4096;
#pragma unroll
      for (int i = 0; i < 4; ++i) __builtin_amdgcn_raw_ptr_buffer_load_lds(rC, R + i * 1024, 16, vC[i], kk(st) * 128, 0, 0);
    };
    auto issue_B = [&](int st) { linear(IC<4>{}, smem + 32768 + ((st + 3) & 3) * 16384 + lw * 4096, vL, sB + kk(st) * 32768); };
    auto issue_T = [&](int d, int part) {
      const int sl = d % 5; int kq = k3 + 4 * d; kq -= kq >= ND ? ND : 0;
      if (part & 1) linear(IC<4>{}, smem + (sl == 0 ? 7 : sl == 1 ? 6 : sl == 2 ? 1 : sl == 3 ? 8 : 9) * 16384 + lw * 4096, vL, sHb + kq * 4096);
      LAS char* R = smem + (sl ? 32768 + (sl & 3) * 16384 : 0) + lw * 4096;
#pragma unroll
      for (int i = 0; i < 4; ++i) if (part & 2) __builtin_amdgcn_raw_ptr_buffer_load_lds(rD, R + i * 1024, 16, vD[i], kq * 32768, 0, 0);
    };
    auto issue_E = [&](int e, int part) {
      const int kq = ND + 4 * ks + ((2 * e + (lw >> 1) + tn) & 3);
      if (part & 1) {
        const int so = WS_HB + ((tm * 2 + (((ks >> 1) ^ lw) & 1)) * 43 + kq) * 4096;
        sfor<0, 4>([&](auto Ic) LAM {
          constexpr int i = decltype(Ic)::value;
          __builtin_amdgcn_raw_ptr_buffer_load_lds(rW, smem + (8 + e) * 16384 + (lw & 1) * 8192 + (lw >> 1) * 1024 + i * 1024, 16, vL, so, i * 1024, 16);
        });
      }
      const int so = kq * 32768 + (lw & 1) * (128 - (ks & 1) * 256);
#pragma unroll
      for (int i = 0; i < 4; ++i) if (part & 2) __builtin_amdgcn_raw_ptr_buffer_load_lds(rD, smem + (e ? 2 : 5) * 16384 + lw * 4096 + i * 1024, 16, vD[i], so, 0, 0);
    };
    const int sel[4] = {SEL0, SEL1, SEL2, SEL3}, selh = SELH;
    u32x4 hv[6];
    u64 tag;
    unsigned tch = 0;
    constexpr int P[4] = {PA0, PA1, PA2, PA3}, S[4] = {SA0, SA1, SA2, SA3};
    auto leave = [&](auto Kc, auto Lc, auto Nc) LAM {
      constexpr int K = decltype(Kc)::value, L = decltype(Lc)::value, N = decltype(Nc)::value;
      int n = 0;
      if constexpr (!HDIR && K >= PH && L <= PH + 3 * SH) n += PH + SH * selh >= L && PH + SH * selh <= K;
      sfor<0, 4>([&](auto Gc) LAM {
        constexpr int G = decltype(Gc)::value;
        if constexpr (K >= P[G] && L <= P[G] + 4 * S[G]) n += P[G] + S[G] * sel[G] >= L && P[G] + S[G] * sel[G] <= K;
      });
      if (n == 0) wait_vm<N>(); else if (n == 1) wait_vm<N + 4>(); else if (n == 2) wait_vm<N + 8>(); else wait_vm<N + 12>();
    };
    auto fin = [&](auto Kc, auto Lc, auto Nc) LAM {
      constexpr int K = decltype(Kc)::value, L = decltype(Lc)::value, N = decltype(Nc)::value;
      if constexpr (!HDIR && K >= PH && K <= PH + 3 * SH) {
        if (K == PH + SH * selh) {
          const int sHp = WS_HB + ((m1 * 2 + (lw >> 1)) * 43 + (blk0 >> 1)) * 4096 + (lw & 1) * 2048;
#pragma unroll
          for (int t = 0; t < 6; ++t)
            if (t < 4 || big) __builtin_amdgcn_raw_buffer_store_b128(hv[t], rW, vL + (t & 1) * 1024, sHp + (t >> 1) * 4096, 16);
          sent();
        }
      }
      if constexpr (PT && K == PT)
        asm volatile("buffer_load_dword %0, %1, %2, 0 offen" : "+v"(tch) : "v"(TC1 < 0 && lane >= 32 ? OOB : (tm * 128 + lw * 32 + (lane & 31)) * 4096 + (ks & 1) * 2048 + (lane < 32 ? kk(TC0) : kk(TC1 < 0 ? 0 : TC1)) * 128), "s"(rC) : "memory");
      sfor<0, 4>([&](auto Gc) LAM {
        constexpr int G = decltype(Gc)::value;
        if constexpr (K >= P[G] && K <= P[G] + 4 * S[G]) {
          if (K == P[G] + S[G] * sel[G]) {
            constexpr int R = (ND + 7 + TWN) / TWN, r0 = (G - TWG) * R;
            if constexpr (G >= TWG && G < TWG + TWN) {
              const int t = r0 + lane;
              const int a = t < 8 ? (ND * 32 + ks * 128 + idx * 4 + (t >> 1)) * 1024 + (t & 1) * 128 : (idx * ND + t - 8) * 1024 + (ks & 1) * 128;
              asm volatile("buffer_load_dword %0, %1, %2, 0 offen" : "+v"(tch) : "v"(lane < R && t < ND + 8 ? a + lw * 256 : OOB), "s"(rD) : "memory");
            }
            issue_A(4 * G);
          }
        }
      });
      if constexpr (N >= 0) leave(Kc, Lc, Nc);
      if constexpr (!HDIR && L - 1 >= PH && L - 1 <= PH + 3 * SH && K > 16) {
        if (L - 1 == PH + SH * selh && lane == 0) __hip_atomic_store((gu64*)(ws + WS_FH) + ((m1 * 16 + n1) * 4 + lw), tag, RLX_AGENT);
      }
    };
    wait_vm<4>(); LBAR();
    if (U0E) issue_u(0);
    issue_w(1);
    const char* wse = ws;
    asm volatile("" : "+s"(wse));
    u64 epoch = *(const __attribute__((address_space(4))) u64*)(wse + WS_E);
    sfor<0, 6>([&](auto Kc) LAM {
      constexpr int k = decltype(Kc)::value;
      issue_x(k + 2); issue_w(k + 2);
      if (k == 0) asm volatile("" : "+s"(epoch));
      fin(Kc, IC<k - 1>{}, IC<9>{}); LBAR();
    });
    if (!U0E) issue_u(0);
    issue_u(1); fin(IC<6>{}, IC<5>{}, IC<U0E ? 3 : 6>{}); LBAR();
    issue_u(2); issue_u(3); issue_u(4); fin(IC<7>{}, IC<6>{}, IC<12>{}); LBAR();
    tag = epoch * 6364136223846793005ull + 1442695040888963407ull;
    const gu64* fa = (const gu64*)(ws + WS_FA) + ((((ks & 1) * 8 + l3) * 16 + (ks >> 1) * 8 + tn * 2 + ((lane >> 2) & 1)) * 4 + (lane & 3));
    u64 pv;
    fin(IC<8>{}, IC<6>{}, IC<9>{}); LBAR();
    issue_u(5); issue_u(6); fin(IC<9>{}, IC<7>{}, IC<12>{}); LBAR();
    issue_u(7); fin(IC<10>{}, IC<7>{}, IC<9>{}); LBAR();
    issue_A(1); fin(IC<11>{}, IC<10>{}, IC<4>{}); LBAR();
    asm volatile("global_load_dwordx2 %0, %1, off sc1" : "=v"(pv) : "v"(fa) : "memory");
    const bool first = (l3 >> 1) == (tm & 3);
    issue_A(2);
    asm volatile("s_waitcnt vmcnt(4)" : "+v"(pv) :: "memory");
    bool early = __all(!first || pv == tag);
    if (NPOLL > 1 && !early) {
      asm volatile("global_load_dwordx2 %0, %1, off sc1\n\ts_waitcnt vmcnt(0)" : "=v"(pv) : "v"(fa) : "memory");
      early = __all(!first || pv == tag);
    }
    if (early) issue_B(0);
    fin(IC<12>{}, IC<12>{}, IC<-1>{}); LBAR();
    issue_A(3);
    if (!early) { spin_flags(fa, first, tag); issue_B(0); }
    STAMP(1);
    issue_B(1); fin(IC<13>{}, IC<13>{}, IC<-1>{});
    if (early) leave(IC<13>{}, IC<13>{}, IC<8>{}); else leave(IC<13>{}, IC<13>{}, IC<4>{});
    LBAR();
#pragma unroll
    for (int t = 0; t < 6; ++t) hv[t] = *(LAS const u32x4*)(smem + (t < 4 ? 65536 + t * 1024 : 45056 + t * 1024) + lw * 4096 + lane * 16);
    asm volatile("global_load_dwordx2 %0, %1, off sc1" : "=v"(pv) : "v"(fa) : "memory");
    asm volatile("s_waitcnt lgkmcnt(0)" : "+v"(hv[0]), "+v"(hv[1]), "+v"(hv[2]), "+v"(hv[3]), "+v"(hv[4]), "+v"(hv[5]) :: "memory");
    issue_B(2); fin(IC<14>{}, IC<13>{}, IC<5>{}); LBAR();
    issue_B(3); leave(IC<14>{}, IC<14>{}, IC<8>{});
    asm volatile("" : "+v"(pv) :: "memory");
    if (!__all(pv == tag)) spin_flags(fa, true, tag);
    issue_B(4); fin(IC<15>{}, IC<14>{}, IC<8>{}); LBAR();
    issue_A(5); issue_B(5); fin(IC<16>{}, IC<15>{}, IC<12>{}); LBAR();
    const gu64* fh = (const gu64*)(ws + WS_FH) + (tm * 64 + lane);
    sfor<2, 12>([&](auto Jc) LAM {
      constexpr int j = decltype(Jc)::value;
      if constexpr (j == 9) asm volatile("global_load_dwordx2 %0, %1, off sc1" : "=v"(pv) : "v"(fh) : "memory");
      if constexpr (j & 3) issue_A(j + 4);
      issue_B(j + 4);
      if constexpr (j == 8) issue_T(0, 2);
      fin(IC<15 + j>{}, IC<13 + j>{}, IC<((j & 3) == 0 || (j & 3) == 1 ? 12 : 16) + (j == 9 || j == 10) + (j == 8 || j == 9 ? 4 : 0)>{});
      if constexpr (j == 11) asm volatile("" : "+v"(pv) :: "memory");
      LBAR();
    });
    asm volatile("" :: "v"(tch));
    issue_E(0, 2);
    if (!__all(pv == tag)) spin_flags(fh, true, tag);
    STAMP(2);
    sfor<1, 12>([&](auto Ic) LAM {
      constexpr int i = decltype(Ic)::value, d = i - 5;
      constexpr int DCUT[12] = {1, DSCHED}, lo = DCUT[i - 1], hi = DCUT[i];
      sfor<lo, hi>([&](auto Hc) LAM {
        constexpr int h = decltype(Hc)::value;
        if (h == 15 && lw == 3) {
          vL = OOB;
#pragma unroll
          for (int q = 0; q < 4; ++q) vD[q] = OOB;
        }
        if constexpr (h < 2) issue_E(0, 1); else if constexpr (h == 2) issue_T(0, 1); else if constexpr (h < 5) issue_E(1, h - 2);
        else issue_T((h - 3) / 2, h < HFIRST ? 1 + (h & 1) : 2 - (h & 1));
      });
      constexpr int need = i <= 3 ? i - 1 : d < 0 ? 5 : 5 + 2 * d;
      static_assert(hi <= (d <= 1 ? 2 * i + 1 : 2 * d + 9) && hi >= need, "slot free / step issued");
      if (i == 11) STAMP(3);
      wait_vm<4 * (hi - need) + (i == 1 ? 8 : 0)>();
      LBAR();
    });
    STAMP_OUT();
    if (b == 0 && wid == 7) {
#pragma unroll 1
      for (int q = 0; q < 4; ++q) spin_flags((const gu64*)(ws + WS_FA) + (q * 64 + lane) * 4, true, tag);
      if (lane == 0) __hip_atomic_store((gu64*)(ws + WS_E), tag, RLX_AGENT);
    }
  } else {
    const int w = wid;
    const char* wse = ws;
    asm volatile("" : "+s"(wse));
    const u64 epoch = *(const __attribute__((address_space(4))) u64*)(wse + WS_E);
    const u64 tag = epoch * 6364136223846793005ull + 1442695040888963407ull;
    auto rW = __builtin_amdgcn_make_buffer_rsrc((void*)ws, 0, WS_END, 0x00020000);
    const unsigned lds0 = (unsigned)(size_t)smem;
    auto rd128 = [&](unsigned a, int o) { return *(LAS const bf16x8*)((LAS char*)(size_t)a + o); };
    auto rdtr = [&](unsigned a, int o, int d) {
      bf16x4 lo = __builtin_amdgcn_ds_read_tr16_b64_v4bf16((LAS bf16x4*)((LAS char*)(size_t)a + o));
      bf16x4 hi = __builtin_amdgcn_ds_read_tr16_b64_v4bf16((LAS bf16x4*)((LAS char*)(size_t)a + o + d));
      return __builtin_shufflevector(lo, hi, 0, 1, 2, 3, 4, 5, 6, 7);
    };
    const int wm = w >> 1, wn = w & 1;
    unsigned aX[2], aD0[2];
    const unsigned vl = opq(lane * 16);
    aD0[0] = opq(lds0 + (lane >> 2) * 128 + (((2 * wn) ^ ((lane >> 3) & 3)) * 32) + (lane & 3) * 8 + 32768);
    int sY = WS_YP + wgl * 24576 + w * 2048;
    asm volatile("" : "+s"(sY));
    auto stage1 = [&](auto NBc) LAM {
      constexpr int NB = decltype(NBc)::value, NF = NB == 4 ? 9 : 10, D = 4;
      const int i16 = lane & 15, g = lane >> 4;
      unsigned aH[2], aW[2];
#pragma unroll
      for (int s = 0; s < 2; ++s) {
        aH[s] = opq(lds0 + i16 * 128 + (((4 * s + g) ^ ((i16 >> 1) & 7)) * 16)); aX[s] = opq(aH[s] + w * 4096);
        aW[s] = opq(lds0 + (8 * g + (i16 >> 2)) * 192 + ((s ^ (g & 1)) * 32) + (lane & 3) * 8 + wslot(0));
      }
      const unsigned aG = opq(lds0 + g * 16 + 159744);
      bf16x8 xf[16][2], fr[8];
      f32x4 G[NB][2];
      WAIT_LGKM0();
      BARRIER();
      STAMP(1);
      {
        f32x4 ab[2][4], z[2];
        auto rdF = [&](auto Fc) LAM {
          constexpr int F = decltype(Fc)::value, kq = F / NF, j = kq >> 1, s = kq & 1, f = F % NF;
          if (j && !s && !f) {
            constexpr int dl = j % 3 ? SLOT : -2 * SLOT;
#pragma unroll
            for (int t = 0; t < 2; ++t) { aX[t] = opq(aX[t] + dl); aH[t] = opq(aH[t] + dl); aW[t] = opq(aW[t] + (wslot(j) - wslot(j - 1))); }
          }
          if (f == 0) { xf[kq][0] = rd128(aX[s], 20480); xf[kq][1] = rd128(aX[s], 22528); }
          if constexpr (f < 4) fr[F & 7] = rd128(aH[s], f * 2048);
          else if constexpr (f < 4 + NB) fr[F & 7] = rdtr(aW[f & 1], ((f - 4) >> 1) * 64 + s * 6144, 768);
          else fr[F & 7] = rd128(aG, kq * 64);
        };
        auto mm = [&](auto Fc, int mb) LAM {
          constexpr int F = decltype(Fc)::value, kq = F / NF, f = F % NF;
          if constexpr (f < 4) mfx<kq == 0>(ab[mb][f], xf[kq][mb], fr[F & 7]);
          else if constexpr (f < 4 + NB) mfx<kq == 0>(G[f - 4][mb], fr[F & 7], xf[kq][mb]);
          else mfx<kq == 0>(z[mb], fr[F & 7], xf[kq][mb]);
        };
        sfor<0, D>(rdF);
        SB();
        sfor<0, 16 * NF>([&](auto Fc) LAM {
          constexpr int F = decltype(Fc)::value;
          if constexpr (F + D < 16 * NF && ((F + D) % (2 * NF)) == 0) { BARRIER(); SB(); }
          mm(Fc, 0); SB();
          if constexpr (F + D < 16 * NF) rdF(IC<F + D>{});
          SB();
          mm(Fc, 1); SB();
        });
        WAIT_LGKM0(); BARRIER();
        settle();
#pragma unroll
        for (int c = 0; c < 4; ++c) { pin(ab[0][c]); pin(ab[1][c]); }
        if constexpr (NB == 4) { pin(z[0]); pin(z[1]); }
        STAMP(2);
        const unsigned sgn = n1 < 8 ? 0u : 0x80008000u;
        const int sA = WS_AB + (((n1 >> 2) * 64 + m1 * 4 + w) * 16 + (n1 & 3) * 4) * 1024;
#pragma unroll
        for (int c = 0; c < 4; ++c) {
          u32x4 v = {cvtpk(ab[0][c][0], ab[0][c][1]) ^ sgn, cvtpk(ab[0][c][2], ab[0][c][3]) ^ sgn,
                     cvtpk(ab[1][c][0], ab[1][c][1]) ^ sgn, cvtpk(ab[1][c][2], ab[1][c][3]) ^ sgn};
          SB();
          __builtin_amdgcn_raw_buffer_store_b128(v, rW, vl + c * 1024, sA, 16);
          sent();
        }
        if (NB == 4 && n1 == 3 && lane < 16) {
#pragma unroll
          for (int mb = 0; mb < 2; ++mb) {
            float gv = __builtin_amdgcn_rcpf(1.0f + __builtin_amdgcn_exp2f(-1.4426950409f * z[mb][0]));
            __hip_atomic_store((gu32*)(ws + WS_G) + m1 * 128 + w * 32 + lane * 2 + mb, __builtin_bit_cast(unsigned, gv), RLX_AGENT);
          }
        }
        SB();
      }
      STAMP(3);
      {
        gu64* const fa = (gu64*)(ws + WS_FA) + ((m1 * 16 + n1) * 4 + w);
        f32x4 U[NB][2];
        float t, r;
        auto rdF = [&](auto Fc) LAM {
          constexpr int F = decltype(Fc)::value, kq = F / NB, j = kq >> 1, s = kq & 1, f = F % NB;
          if ((j == 0 || j == 1 || j == 4) && !s && !f) {
            constexpr int dl = j == 0 ? 147456 - wslot(7) : -73728;
#pragma unroll
            for (int c = 0; c < 2; ++c) aW[c] = opq(aW[c] + dl);
          }
          if (kq >= 14 && f == 0) { xf[kq][0] = rd128(aX[s], 20480); xf[kq][1] = rd128(aX[s], 22528); }
          fr[F & 7] = rdtr(aW[f & 1], (j == 0 ? 0 : j < 4 ? j - 1 : j - 4) * 12288 + (f >> 1) * 64 + s * 6144, 768);
        };
        auto act = [&](auto Fc) LAM {
          constexpr int F = decltype(Fc)::value, v = F >> 2, ph = F & 3;
          if constexpr (ph == 0) asm volatile("v_fmaak_f32 %0, %1, %1, 0x41b2e92f\n\tv_mul_f32 %0, %1, %0" : "=&v"(t) : "v"(G[v >> 2][0][v & 3]));
          if constexpr (ph == 1) {
            asm volatile("v_mul_f32 %0, 0xbdd2d3e8, %0" : "+v"(t));
            if constexpr (v > 0) asm volatile("v_mul_f32 %0, %0, %1" : "+v"(G[(v - 1) >> 2][0][(v - 1) & 3]) : "v"(r));
          }
          if constexpr (ph == 2) asm volatile("v_exp_f32 %0, %0" : "+v"(t));
          if constexpr (ph == 3) asm volatile("v_add_f32 %1, 1.0, %1\n\tv_rcp_f32 %0, %1" : "=v"(r), "+v"(t));
        };
        sfor<0, D>(rdF);
        SB();
        sfor<0, 16 * NB>([&](auto Fc) LAM {
          constexpr int F = decltype(Fc)::value, f = F % NB;
          if (F + D == FA_KQ * NB) { wait_vm<0>(); if (lane == 0) __hip_atomic_store(fa, tag, RLX_AGENT); SB(); }
          if constexpr (((F + D) % (2 * NB)) == 0 && ((0x2e >> ((F + D) / (2 * NB))) & 1)) { BARRIER(); SB(); }
          mfx<F / NB == 0>(U[f][0], fr[F & 7], xf[F / NB][0]); SB();
          if constexpr (F + D < 16 * NB) rdF(IC<F + D>{});
          SB();
          mfx<F / NB == 0>(U[f][1], fr[F & 7], xf[F / NB][1]); SB();
          act(Fc); SB();
        });
        WAIT_LGKM0(); BARRIER();
        settle();
        asm volatile("v_mul_f32 %0, %0, %1" : "+v"(G[NB - 1][0][3]) : "v"(r));
#pragma unroll
        for (int c = 0; c < NB; ++c) { pin(U[c][0]); pin(U[c][1]); }
        STAMP(4);
        XS(8);
        LAS char* const hs = (LAS char*)(size_t)opq(lds0 + w * 4096 + 49152 + vl);
        f32x2 c1 = {-2.302208198f, -2.302208198f}, c2 = {-0.1029432397f, -0.1029432397f};
        asm volatile("" : "+v"(c1), "+v"(c2));
        u32x4 hp[NB / 2][2];
        auto half0 = [&](int i) {
          const f32x4 h0 = G[2 * i][0] * U[2 * i][0], h1 = G[2 * i + 1][0] * U[2 * i + 1][0];
          hp[i][0] = u32x4{cvtpk(h0[0], h0[1]), cvtpk(h0[2], h0[3]), cvtpk(h1[0], h1[1]), cvtpk(h1[2], h1[3])};
          SB();
        };
        auto half1 = [&](int i) {
          const f32x4 g0 = G[2 * i][1], u0 = U[2 * i][1], g1 = G[2 * i + 1][1], u1 = U[2 * i + 1][1];
          hp[i][1] = u32x4{geglu(g0.xy, u0.xy, c1, c2), geglu(g0.zw, u0.zw, c1, c2), geglu(g1.xy, u1.xy, c1, c2), geglu(g1.zw, u1.zw, c1, c2)};
          SB();
        };
        const int sHp = WS_HB + ((m1 * 2 + (w >> 1)) * 43 + (blk0 >> 1)) * 4096 + (w & 1) * 2048;
        auto put = [&](int i, int mb) {
          if (HDIR) __builtin_amdgcn_raw_buffer_store_b128(hp[i][mb], rW, vl + mb * 1024, sHp + i * 4096, 16);
          else *(LAS u32x4*)(hs + (i < 2 ? 16384 + i * 2048 : 0) + mb * 1024) = hp[i][mb];
          if (HDIR) sent(); else SB();
        };
#pragma unroll
        for (int i = 0; i < NB / 2; ++i) {
          if (HORD == 0) { half0(i); put(i, 0); half1(i); put(i, 1); }
          if (HORD == 1) { half0(i); half1(i); }
          if (HORD == 2) { half1(i); put(i, 1); half0(i); if (i) put(i - 1, 0); }
          XS(9 + i);
        }
        if (HORD == 1) for (int i = 0; i < NB; ++i) put(i >> 1, i & 1);
        if (HORD == 2) put(NB / 2 - 1, 0);
        XS(13);
        WAIT_LGKM0();
        XS(14);
        SB();
      }
    };
    if (big) stage1(IC<6>{}); else stage1(IC<4>{});
    STAMP(5);
    unsigned aH0, aH1, aD1[2];
    bf16x8 hf[4][2], df[4][2];
    auto rh = [&](int sl, int j, int mb) { hf[j][mb] = rd128(sl == 2 ? aH0 : aH1, (sl == 1 ? 0 : sl < 3 ? 1 : sl - 1) * 16384 + j * 4096 + mb * 1024); };
    auto rd = [&](int sl, int j, int c) { df[j][c] = rdtr(sl == 0 ? aD1[c] : aD0[c], (sl & 3) * 16384 + j * 4096, 2048); };
    u32x4 yq[6];
    f32x4 own[2][2];
    u32x4 pick[2];
    int vO, sZ[3];
    gu64* fy; const gu64* fz;
    float sc[64];
    f32x4 ac2[4][4];
    f32x2 gv[2], gn[2];
    {
      unsigned aA[2], aL[2], aZ[2];
#pragma unroll
      for (int s = 0; s < 2; ++s) aZ[s] = opq(aX[s] + (114688 - SLOT + (wm - w) * 4096));
      const unsigned aB = opq(vl + (lds0 + wn * 2048 + 32768));
      f32x4 acc[4][4];
      bf16x8 fa[2][4], fb[2][4];
      auto rdA = [&](int set, int mb, int slot, int s) { fa[set][mb] = rd128(slot == 0 ? aZ[s] : slot == 3 ? aL[s] : aA[s], (slot == 1 ? 49152 : slot == 3 ? 16384 : 0) + (mb >> 1) * 8192 + (mb & 1) * 2048); };
      auto rdB = [&](int set, int nb, int slot, int s) { fb[set][nb] = rd128(aB, ((slot + 3) & 3) * 16384 + s * 8192 + (nb >> 1) * 4096 + (nb & 1) * 1024); };
      auto half = [&](int u, int s) LAM {
        const int cur = s, nxt = s ^ 1, ns = s ^ 1, nslot = s ? ((u + 1) & 3) : u;
        auto mm = [&](int nb, int mb) LAM { mfa(acc[nb][mb], fb[cur][nb], fa[cur][mb]); SB(); };
        if (s == 1) { WAIT_LGKM0(); BARRIER(); SB(); }
        mm(0, 0); rdA(nxt, 0, nslot, ns); SB();
        mm(1, 0); rdB(nxt, 0, nslot, ns); SB();
        mm(2, 0); rdB(nxt, 1, nslot, ns); SB();
        mm(3, 0); rdB(nxt, 2, nslot, ns); SB();
        mm(0, 1); rdB(nxt, 3, nslot, ns); SB();
        mm(1, 1); rdA(nxt, 1, nslot, ns); SB();
        mm(2, 1); rdA(nxt, 2, nslot, ns); SB();
        mm(3, 1); rdA(nxt, 3, nslot, ns); SB();
        mm(0, 2); mm(1, 2); mm(2, 2); mm(3, 2);
        mm(0, 3); mm(1, 3); mm(2, 3); mm(3, 3);
      };
      XS(15);
      BARRIER();
      STAMP(6);
      rdA(0, 0, 0, 0);
#pragma unroll
      for (int i = 0; i < 4; ++i) rdB(0, i, 0, 0);
#pragma unroll
      for (int i = 1; i < 4; ++i) rdA(0, i, 0, 0);
      SB();
#pragma unroll
      for (int s = 0; s < 2; ++s) { aL[s] = opq(aZ[s] - 114688); aA[s] = opq(aZ[s] - 16384); }
      aH0 = opq(vl + (lds0 + wm * 2048)); aH1 = opq(aH0 + 98304);
      aD0[1] = opq(aD0[0] ^ 32); aD1[0] = opq(aD0[0] - 32768); aD1[1] = opq(aD0[1] - 32768);
#pragma unroll
      for (int i = 0; i < 16; ++i) { acc[i >> 2][i & 3] = f32x4{}; pin(acc[i >> 2][i & 3]); }
      bf16x4 fw[8];
      asm volatile("" : "=v"(gv[0]), "=v"(gv[1]), "=v"(fw[0]), "=v"(fw[1]), "=v"(fw[2]), "=v"(fw[3]), "=v"(fw[4]), "=v"(fw[5]), "=v"(fw[6]), "=v"(fw[7]));
      const unsigned aE = opq(aH0 + wm * 2048 + 131072), vG = opq((vl & 240) >> 1);
      {
        const int lane = vl >> 4, g = lane >> 4, q = (lane & 15) >> 2, p = lane & 3;
        const unsigned one = g == q ? 0x3f80u << (16 * (p & 1)) : 0u, o0 = p < 2 ? one : 0u, o1 = one - o0;
        pick[0] = u32x4{o0, o1, 0u, 0u}; pick[1] = u32x4{0u, 0u, o0, o1};
        vO = ((tm * 128 + (ks >> 1) * 64 + wm * 32 + 8 * q + p) * 512 + tn * 128 + (ks & 1) * 64 + wn * 32 + (g & 1) * 16 + (g >> 1) * 8) * 2;
        fy = (gu64*)(ws + WS_FY) + (wgl * 4 + w) * 8;
        fz = (const gu64*)(ws + WS_FY) + ((((xcd & 4) | p) * 32 + idx) * 4 + w) * 8;
#pragma unroll
        for (int o = 0; o < 3; ++o) sZ[o] = WS_YP + (((xcd ^ (o + 1)) * 32 + idx) * 3 + o) * 8192 + w * 2048;
        asm volatile("" : "+v"(pick[0]), "+v"(pick[1]), "+v"(vO), "+s"(fy), "+v"(fz), "+s"(sZ[0]), "+s"(sZ[1]), "+s"(sZ[2]));
      }
      SB();
#pragma unroll 1
      for (int it = 0; it < 4; ++it) {
        if (HDIR && it == FH_IT) { wait_vm<0>(); if (vl == 0) __hip_atomic_store((gu64*)(ws + WS_FH) + ((m1 * 16 + n1) * 4 + w), tag, RLX_AGENT); SB(); }
        half(0, 0); half(0, 1); half(1, 0); half(1, 1);
        { const unsigned z = it == 0 ? 131072 : it == 1 ? 0 : 114688; aZ[0] = opq(aL[0] + z); aZ[1] = opq(aL[1] + z); SB(); }
        if (it == 3) {
          const int sG = WS_G + (tm * 128 + (ks >> 1) * 64 + wm * 32) * 4;
          asm volatile("buffer_load_dwordx2 %0, %3, %4, %5 offen sc1\n\tbuffer_load_dwordx2 %1, %3, %4, %6 offen sc1\n\tv_mov_b32 %2, %7"
                       : "+v"(gv[0]), "+v"(gv[1]), "+v"(aZ[0]) : "v"(vG), "s"(rW), "s"(sG), "s"(sG ^ 256), "v"(aE) : "memory");
          SB();
        }
        half(2, 0); half(2, 1);
        if (it == 3) {
#pragma unroll
          for (int i = 0; i < 8; ++i)
            asm volatile("ds_read_b64_tr_b16 %0, %1 offset:%2" : "+v"(fw[i]) : "v"(aD0[(i >> 1) & 1]), "n"(49152 + (i >> 2) * 4096 + (i & 1) * 2048) : "memory");
          SB();
        }
        half(3, 0); half(3, 1);
      }
      STAMP(7);
      XS(0);
      auto rdE = [&](int set, int mb, int e, int s) { fa[set][mb] = rd128(aE, e * 16384 + s * 1024 + (mb >> 1) * 8192 + (mb & 1) * 2048); };
      auto rdW = [&](int set, int nb, int e, int s) { fb[set][nb] = rdtr(aD0[nb & 1], (e ? 0 : 49152) + s * 8192 + (nb >> 1) * 4096, 2048); };
#pragma unroll
      for (int i = 0; i < 4; ++i) fb[0][i] = __builtin_shufflevector(fw[2 * i], fw[2 * i + 1], 0, 1, 2, 3, 4, 5, 6, 7);
      SB();
      XS(1);
      sfor<0, 4>([&](auto Hc) LAM {
        constexpr int H = decltype(Hc)::value, cur = H & 1, nxt = cur ^ 1, e = (H + 1) >> 1;
        auto mm = [&](int nb, int mb) LAM { mfx<H == 0>(ac2[nb][mb], fb[cur][nb], fa[cur][mb]); SB(); };
        auto ra = [&](int i) LAM { if (H < 3) rdE(nxt, i, e, nxt); else rh(0, i >> 1, i & 1); SB(); };
        auto rb = [&](int i) LAM { if (H < 3) rdW(nxt, i, e, nxt); else rd(0, i >> 1, i & 1); SB(); };
        auto gs = [&](int i, int c) LAM {
          const int n = H * 20 - 8 + i - GLATE;
#pragma unroll
          for (int k = n; k < n + c; ++k)
            if (k >= 0 && k < 64) asm volatile("v_mul_f32 %0, %1, %0" : "+v"(sc[k]) : "v"(gv[(k >> 3) & 1][(k >> 2) & 1]));
          SB();
        };
        auto gw = [&]() LAM {
          asm volatile("s_waitcnt vmcnt(0)" : "+v"(gv[0]), "+v"(gv[1]) :: "memory");
#pragma unroll
          for (int i = 0; i < 16; ++i) pin(acc[i >> 2][i & 3]);
#pragma unroll
          for (int i = 0; i < 64; ++i) sc[i] = acc[i >> 4][(i >> 2) & 3][i & 3];
          SB();
        };
        if (cur == 1) { WAIT_LGKM0(); BARRIER(); SB(); }
        if (H == 1 && GLATE) gw();
        mm(0, 0); ra(0); gs(0, 1);
        mm(1, 0); rb(0);
        mm(2, 0); rb(1);
        mm(3, 0); rb(2);
        mm(0, 1); rb(3);
        mm(1, 1); ra(1); gs(1, 1);
        mm(2, 1); ra(2); gs(2, 1);
        mm(3, 1); ra(3); gs(3, 1);
        mm(0, 2); gs(4, 2);
        mm(1, 2); gs(6, 2);
        if (H == 0 && !GLATE) gw();
        mm(2, 2); gs(8, 2);
        mm(3, 2); gs(10, 2);
        mm(0, 3); gs(12, 2);
        mm(1, 3); gs(14, 2);
        mm(2, 3); gs(16, 2);
        mm(3, 3); gs(18, 2);
      });
#pragma unroll
      for (int k = 72 - GLATE; k < 64; ++k) asm volatile("v_mul_f32 %0, %1, %0" : "+v"(sc[k]) : "v"(gv[(k >> 3) & 1][(k >> 2) & 1]));
      gn[0] = 1.0f - gv[0]; gn[1] = 1.0f - gv[1];
      asm volatile("" : "+v"(gn[0]), "+v"(gn[1]));
      SB();
      settle();
#pragma unroll
      for (int i = 0; i < 16; ++i) {
        pin(ac2[i >> 2][i & 3]);
        if (i & 10) acc[i >> 2][i & 3] = f32x4{sc[4 * i], sc[4 * i + 1], sc[4 * i + 2], sc[4 * i + 3]} + gn[(i >> 1) & 1][i & 1] * ac2[i >> 2][i & 3];
      }
#pragma unroll
      for (int i = 0; i < 6; ++i) {
        const int nb = ((i >> 1) + 1 & 1) * 2, mb = ((i >> 1) + 1 & 2) + (i & 1);
        yq[i] = u32x4{cvtpk(acc[nb][mb][0], acc[nb][mb][1]), cvtpk(acc[nb][mb][2], acc[nb][mb][3]),
                      cvtpk(acc[nb + 1][mb][0], acc[nb + 1][mb][1]), cvtpk(acc[nb + 1][mb][2], acc[nb + 1][mb][3])};
      }
      SB();
      XS(2);
#pragma unroll
      for (int i = 0; i < 6; ++i) if (YPOS(i) < 0) __builtin_amdgcn_raw_buffer_store_b128(yq[i], rW, vl + (i & 1) * 1024, sY + (i >> 1) * 8192, 16);
      sent();
    }
    STAMP(8);
    XS(3);
    {
      const int lane = vl >> 4;
      f32x4 acc[2][2];
      u32x4 y[3][2];
      const bool fzl = (lane & 3) != ks;
      u64 pv; bool late = true;
      auto rO = __builtin_amdgcn_make_buffer_rsrc((void*)out, 0, 2048 * 512 * 2, 0x00020000);
      asm volatile("" : "+s"(rO));
      auto load_y = [&]() {
#pragma unroll
        for (int o = 0; o < 3; ++o) {
#pragma unroll
          for (int mb = 0; mb < 2; ++mb)
            asm volatile("buffer_load_dwordx4 %0, %1, %2, %3 offen offset:%4 sc1" : "=v"(y[o][mb]) : "v"(vl), "s"(rW), "s"(sZ[o]), "n"(mb * 1024) : "memory");
        }
      };
      auto kstep = [&](auto Fc, int j, int sl, int jn) LAM {
        constexpr bool F = decltype(Fc)::value;
        mfx<F>(acc[0][0], df[j][0], hf[j][0]); SB(); if (jn >= 0) rd(sl, jn, 0); SB();
        mfx<F>(acc[1][0], df[j][1], hf[j][0]); SB(); if (jn >= 0) { rh(sl, jn, 0); rh(sl, jn, 1); } SB();
        mfx<F>(acc[0][1], df[j][0], hf[j][1]); SB(); if (jn >= 0) rd(sl, jn, 1); SB();
        mfx<F>(acc[1][1], df[j][1], hf[j][1]); SB();
      };
      auto check = [&]() LAM {
        asm volatile("s_waitcnt vmcnt(0)" : "+v"(pv) :: "memory");
        late = !__all(!fzl || pv == tag);
        if (!late) load_y();
        SB();
      };
      auto put = [&](auto Pc) LAM {
        constexpr int P = decltype(Pc)::value - OWNP;
        if constexpr (P >= 0 && P < 8) {
          constexpr int nb = P >> 2, mb = (P >> 1) & 1, e = (P & 1) * 2, k = nb * 16 + mb * 4 + e;
          asm volatile("v_fmac_f32 %0, %2, %3\n\tv_fmac_f32 %1, %2, %4" : "+v"(sc[k]), "+v"(sc[k + 1]) : "v"(gn[0][mb]), "v"(ac2[nb][mb][e]), "v"(ac2[nb][mb][e + 1]));
          if constexpr (e) own[nb][mb] = f32x4{sc[k - 2], sc[k - 1], sc[k], sc[k + 1]};
          SB();
        }
        sfor<0, 6>([&](auto Ic) LAM {
          constexpr int i = decltype(Ic)::value;
          if constexpr (YPOS(i) == decltype(Pc)::value) { __builtin_amdgcn_raw_buffer_store_b128(yq[i], rW, vl + (i & 1) * 1024, sY + (i >> 1) * 8192, 16); sent(); }
        });
        if constexpr (decltype(Pc)::value == FYK) { wait_vm<0>(); if (lane == 0) __hip_atomic_store(fy, tag, RLX_AGENT); SB(); }
        if constexpr (decltype(Pc)::value == FYP) { asm volatile("global_load_dwordx2 %0, %1, off sc1" : "=v"(pv) : "v"(fz) : "memory"); SB(); }
        if constexpr (decltype(Pc)::value == FYC) check();
      };
      auto sum_y = [&]() LAM {
        asm volatile("s_waitcnt vmcnt(0)" : "+v"(y[0][0]), "+v"(y[0][1]), "+v"(y[1][0]), "+v"(y[1][1]), "+v"(y[2][0]), "+v"(y[2][1]) :: "memory");
#pragma unroll
        for (int i = 0; i < 12; ++i) mfa(own[i & 1][(i >> 1) & 1], __builtin_bit_cast(bf16x8, pick[i & 1]), __builtin_bit_cast(bf16x8, y[i >> 2][(i >> 1) & 1]));
        settle();
#pragma unroll
        for (int i = 0; i < 4; ++i) pin(own[i & 1][i >> 1]);
        SB();
      };
      static_assert(YPOS(5) <= FYK && FYK < FYP && FYP < FYC && FYC < ND, "stores, flag, sample, check");
      sfor<0, 6>([&](auto Dc) LAM {
        constexpr int d = decltype(Dc)::value, sl = d % 5;
        kstep(IC<d == 0>{}, 0, sl, 2); put(IC<4 * d>{}); kstep(IC<0>{}, 1, sl, 3); put(IC<4 * d + 1>{});
        BARRIER(); SB();
        kstep(IC<0>{}, 2, (sl + 1) % 5, 0); put(IC<4 * d + 2>{}); kstep(IC<0>{}, 3, (sl + 1) % 5, 1); put(IC<4 * d + 3>{});
      });
      kstep(IC<0>{}, 0, 1, 2); put(IC<24>{}); kstep(IC<0>{}, 1, 1, -1); put(IC<25>{}); kstep(IC<0>{}, 2, 1, -1); put(IC<26>{});
      settle();
#pragma unroll
      for (int i = 0; i < 4; ++i) { pin(acc[i >> 1][i & 1]); own[i >> 1][i & 1] += gn[0][i & 1] * acc[i >> 1][i & 1]; pin(own[i >> 1][i & 1]); }
      SB();
      STAMP(9);
      if (late) {
        if (PSPIN) {
          u64 p0, p1, p2;
          asm volatile("global_load_dwordx2 %0, %2, off sc1\n\ts_sleep %3\n\tglobal_load_dwordx2 %1, %2, off sc1" : "=&v"(p0), "=&v"(p1) : "v"(fz), "n"(PSPIN) : "memory");
#pragma unroll 1
          for (int s = 0; s < SPIN_CAP; ++s) {
            asm volatile("s_sleep %3\n\tglobal_load_dwordx2 %0, %2, off sc1\n\ts_waitcnt vmcnt(2)" : "=&v"(p2), "+v"(p0) : "v"(fz), "n"(PSPIN) : "memory");
            if (__all(!fzl || p0 == tag)) break;
            asm volatile("s_sleep %3\n\tglobal_load_dwordx2 %0, %2, off sc1\n\ts_waitcnt vmcnt(2)" : "=&v"(p0), "+v"(p1) : "v"(fz), "n"(PSPIN) : "memory");
            if (__all(!fzl || p1 == tag)) break;
            asm volatile("s_sleep %3\n\tglobal_load_dwordx2 %0, %2, off sc1\n\ts_waitcnt vmcnt(2)" : "=&v"(p1), "+v"(p2) : "v"(fz), "n"(PSPIN) : "memory");
            if (__all(!fzl || p2 == tag)) break;
          }
          load_y();
          asm volatile("s_waitcnt vmcnt(6)" : "+v"(p0), "+v"(p1), "+v"(p2) :: "memory");
        } else { spin_flags(fz, fzl, tag); load_y(); }
      }
      sum_y();
      STAMP(10);
#pragma unroll
      for (int mb = 0; mb < 2; ++mb) {
        const f32x4 r0 = own[0][mb], r1 = own[1][mb];
        auto s0 = __builtin_amdgcn_permlane16_swap(cvtpk(r0[0], r0[1]), cvtpk(r1[0], r1[1]), false, false);
        auto s1 = __builtin_amdgcn_permlane16_swap(cvtpk(r0[2], r0[3]), cvtpk(r1[2], r1[3]), false, false);
        u32x4 v = {s0[0], s1[0], s0[1], s1[1]};
        SB();
        __builtin_amdgcn_raw_buffer_store_b128(v, rO, vO, mb * 4096, AUX_O);
        sent();
      }
      STAMP(11);
      STAMP_OUT();
    }
  }
#endif
}

extern "C" void kernel_launch(void* const* d_in, const int* in_sizes, int n_in, void* d_out, int out_size, void* d_ws,
                              size_t ws_size, hipStream_t stream) {
  (void)in_sizes; (void)n_in; (void)out_size; (void)ws_size;
  constexpr int kLds = 163840;
  (void)hipFuncSetAttribute((const void*)fused_kernel, hipFuncAttributeMaxDynamicSharedMemorySize, kLds);
  fused_kernel<<<256, 512, kLds, stream>>>((const u16*)d_in[0], (const u16*)d_in[1], (const u16*)d_in[2], (const u16*)d_in[3],
                                           (const u16*)d_in[4], (const u16*)d_in[6], (const u16*)d_in[7], (const u16*)d_in[8],
                                           (const u16*)d_in[9], (u16*)d_out, (char*)d_ws + WSOFF);
}
```

```cpp
#include <hip/hip_runtime.h>
#include <hip/hip_bf16.h>

typedef unsigned short u16;
typedef unsigned long long u64;
typedef __bf16 bf16x8 __attribute__((ext_vector_type(8)));
typedef __bf16 bf16x4 __attribute__((ext_vector_type(4)));
typedef __bf16 bf16x2 __attribute__((ext_vector_type(2)));
typedef float f32x4 __attribute__((ext_vector_type(4)));
typedef float f32x2 __attribute__((ext_vector_type(2)));
typedef unsigned u32x4 __attribute__((ext_vector_type(4)));
typedef unsigned u32x2 __attribute__((ext_vector_type(2)));
typedef __attribute__((address_space(1))) u64 gu64;
typedef __attribute__((address_space(1))) unsigned gu32;
#define LAS __attribute__((address_space(3)))
#define DI __device__ __forceinline__
#define SB() __builtin_amdgcn_sched_barrier(0)
#define BARRIER() do { asm volatile("" ::: "memory"); __builtin_amdgcn_s_barrier(); asm volatile("" ::: "memory"); } while (0)
#define WAIT_LGKM0() __builtin_amdgcn_s_waitcnt(0xC07F)
#define RLX_AGENT __ATOMIC_RELAXED, __HIP_MEMORY_SCOPE_AGENT
template <int V> struct IC { static constexpr int value = V; };
template <int I, int N, class Fn> DI void sfor(Fn&& fn) { if constexpr (I < N) { fn(IC<I>{}); sfor<I + 1, N>(fn); } }
#define LAM __attribute__((always_inline))
template <int N> DI void wait_vm() { asm volatile("s_waitcnt vmcnt(%0)" :: "n"(N) : "memory"); }

#define K1MODE 2
#define FA_KQ 4
#define PH 14
#define SH 1
#define SELH (tm & 3)
#define FYK 5
#define FYP 14
#define FYC 23
#define PSPIN 0
#define PA0 5
#define SA0 1
#define PA1 9
#define SA1 1
#define PA2 14
#define SA2 1
#define PA3 18
#define SA3 1
#define SPIN_SLEEP 1
#define SEL (tm & 3)
#define SEL0 SEL
#define SEL1 ((tm & 3) == 2 ? 3 : (tm & 3))
#define SEL2 SEL
#define SEL3 SEL
#define EB0 1
#define PT 0
#define TC0 12
#define TC1 -1
#define U0E 0
#define TWG 1
#define TWN 3
#define WSOFF 0
#define DSCHED 2, 5, 7, 9, 11, 11, 13, 15, 17, 17, 17
#define HFIRST 99
#define OWNP 0
#define NPOLL 2
#define HDIR 0
#define FH_IT 1
#define HORD 0
#define AUX_O 16
#define AUX_1 0

constexpr int WS_E = 0, WS_FA = 4096, WS_FH = 12288, WS_G = 20480, WS_FY = 65536;
constexpr int WS_AB = 1 << 20, WS_HB = 5 << 20, WS_YP = 11 << 20, WS_END = 17 << 20;
constexpr int SPIN_CAP = 1 << 20;
constexpr int ND = 27;

#define STAMP(i) do {} while (0)
#define STAMP_OUT() do {} while (0)

#define XS(i) do {} while (0)
#define LBAR() BARRIER()
#define FS(n) do {} while (0)

DI unsigned cvtpk(float lo, float hi) { f32x2 v = {lo, hi}; bf16x2 b = __builtin_convertvector(v, bf16x2); return __builtin_bit_cast(unsigned, b); }
DI float bflo(unsigned v) { return __builtin_bit_cast(float, v << 16); }
DI float bfhi(unsigned v) { return __builtin_bit_cast(float, v & 0xffff0000u); }
DI unsigned geglu(f32x2 x, f32x2 u, f32x2 c1, f32x2 c2) {
  f32x2 z = x * __builtin_elementwise_fma(x * x, c2, c1);
  f32x2 e = {__builtin_amdgcn_exp2f(z[0]), __builtin_amdgcn_exp2f(z[1])};
  e += 1.0f;
  f32x2 r = {__builtin_amdgcn_rcpf(e[0]), __builtin_amdgcn_rcpf(e[1])};
  r *= x * u;
  return cvtpk(r[0], r[1]);
}
DI void mfa(f32x4& c, bf16x8 a, bf16x8 b) { asm volatile("v_mfma_f32_16x16x32_bf16 %0, %1, %2, %0" : "+v"(c) : "v"(a), "v"(b)); }
DI void mf0(f32x4& c, bf16x8 a, bf16x8 b) { asm volatile("v_mfma_f32_16x16x32_bf16 %0, %1, %2, 0" : "=v"(c) : "v"(a), "v"(b)); }
template <bool FIRST> DI void mfx(f32x4& c, bf16x8 a, bf16x8 b) { if constexpr (FIRST) mf0(c, a, b); else mfa(c, a, b); }
DI void settle() { asm volatile("s_nop 7"); }
DI void sent() { SB(); asm volatile("s_nop 1"); SB(); }
DI void pin(f32x4& c) { asm volatile("" : "+v"(c)); }
DI unsigned opq(unsigned a) { asm volatile("" : "+v"(a)); return a; }
DI int lane_id() { int l; asm volatile("v_mbcnt_lo_u32_b32 %0, -1, 0\n\tv_mbcnt_hi_u32_b32 %0, -1, %0" : "=v"(l)); return l; }
DI u64 uni64(u64 v) {
  unsigned lo = __builtin_amdgcn_readfirstlane((unsigned)v), hi = __builtin_amdgcn_readfirstlane((unsigned)(v >> 32));
  return ((u64)hi << 32) | lo;
}
constexpr int SLOT = 36864;
constexpr int wslot(int j) { return (j & 3) == 0 ? 2 * SLOT + 8192 : (j & 3) == 1 ? 8192 : (j & 3) == 2 ? SLOT + 8192 : 131072; }
DI void spin_flags(const gu64* p, bool active, u64 tag) {
#pragma unroll 1
  for (int s = 0; s < SPIN_CAP; ++s) {
    u64 v = tag;
    if (active) v = __hip_atomic_load(p, RLX_AGENT);
    if (__all(v == tag)) break;
    __builtin_amdgcn_s_sleep(SPIN_SLEEP);
  }
}

__global__ __launch_bounds__(512, 2) void fused_kernel(
    const u16* __restrict__ X, const u16* __restrict__ Cr, const u16* __restrict__ Ci, const u16* __restrict__ Hr,
    const u16* __restrict__ Hi, const u16* __restrict__ Wg, const u16* __restrict__ Wu, const u16* __restrict__ Wd,
    const u16* __restrict__ GW, u16* __restrict__ out, char* __restrict__ ws) {
#if defined(__HIP_DEVICE_COMPILE__)
  extern __shared__ __attribute__((aligned(16))) char smem_[];
  LAS char* smem = (LAS char*)smem_;
  const int tid = threadIdx.x, lane = tid & 63;
  const int wid = __builtin_amdgcn_readfirstlane(tid >> 6);
  const int b = blockIdx.x, xcd = b & 7, idx = b >> 3;
  const int m1 = (~xcd & 4) * 2 + (idx & 7), n1 = ((xcd & 3) ^ 2) * 4 + (idx >> 3);
  const int blk0 = 22 * (n1 >> 2) + 6 * (n1 & 3) - (n1 == 15 ? 2 : 0);
  const bool big = ((n1 & 3) != 3) && (n1 != 14);
  const int ks = xcd & 3, tm = (xcd >> 2) * 8 + (idx >> 2), tn = idx & 3;
  const int wgl = xcd * 32 + idx;
  constexpr int OOB = 0x40000000;
  STAMP(0);
  asm volatile("" : "+s"(X), "+s"(Cr), "+s"(Ci), "+s"(Hr), "+s"(Hi), "+s"(Wg), "+s"(Wu), "+s"(Wd), "+s"(GW), "+s"(out), "+s"(ws));

  if (__builtin_expect(wid >= 4, 1)) {
    const int lw = wid - 4;
    auto rX = __builtin_amdgcn_make_buffer_rsrc((void*)X, 0, 2048 * 512 * 2, 0x00020000);
    auto rH = __builtin_amdgcn_make_buffer_rsrc((void*)(n1 < 8 ? Hr : Hi), 0, 512 * 512 * 2, 0x00020000);
    auto rG = __builtin_amdgcn_make_buffer_rsrc((void*)Wg, 0, 512 * 1376 * 2, 0x00020000);
    auto rU = __builtin_amdgcn_make_buffer_rsrc((void*)Wu, 0, 512 * 1376 * 2, 0x00020000);
    const int l3 = lane >> 3, l7 = lane & 7;
    constexpr int KM = (1 << K1MODE) - 1;
    const int k1 = K1MODE == 3 ? (m1 & 7) + 2 * (n1 & 3) : m1 + n1;
    auto kblk = [&](int j) { return (j & ~KM) | ((j + k1) & KM); };
    int vX[4], vH[2], vW[3];
#pragma unroll
    for (int i = 0; i < 4; ++i) {
      const int v = 32 * lw + 8 * i + l3;
      const int s = 32 * lw + 16 * (i & 1) + 8 * (lane >> 5) + 4 * (i >> 1) + (l3 & 3);
      vX[i] = (m1 * 128 + s) * 1024 + ((l7 ^ ((v >> 1) & 7)) * 16);
    }
    auto issue_x = [&](int j) {
#pragma unroll
      for (int i = 0; i < 4; ++i)
        __builtin_amdgcn_raw_ptr_buffer_load_lds(rX, smem + j % 3 * SLOT + 20480 + (lw * 4 + i) * 1024, 16, vX[i], kblk(j) * 128, 0, AUX_1);
    };
    FS(0);
    issue_x(0);
    FS(1);
#pragma unroll
    for (int i = 0; i < 2; ++i) {
      const int r = 16 * lw + 8 * i + l3;
      vH[i] = ((n1 & 7) * 64 + r) * 1024 + ((l7 ^ ((r >> 1) & 7)) * 16);
    }
#pragma unroll
    for (int i = 0; i < 3; ++i) {
      const int e = i * 64 + lane, rr = (e * 171) >> 11, s = e - 12 * rr, c = (((s >> 1) ^ (rr >> 3)) << 1) | (s & 1);
      vW[i] = (!big && c >= 8) ? OOB : (16 * lw + rr) * 2752 + blk0 * 32 + c * 16;
    }
    auto issue_w = [&](int j) {
      LAS char* R = smem + j % 3 * SLOT;
#pragma unroll
      for (int i = 0; i < 2; ++i) __builtin_amdgcn_raw_ptr_buffer_load_lds(rH, R + (lw * 2 + i) * 1024, 16, vH[i], kblk(j) * 128, 0, AUX_1);
#pragma unroll
      for (int i = 0; i < 3; ++i) __builtin_amdgcn_raw_ptr_buffer_load_lds(rG, smem + wslot(j) + (lw * 3 + i) * 1024, 16, vW[i], kblk(j) * (64 * 2752), 0, AUX_1);
    };
    auto issue_u = [&](int j) {
      LAS char* R = smem + (j == 0 ? 147456 : j < 4 ? 61440 + j * 12288 : (j - 4) * 12288);
#pragma unroll
      for (int i = 0; i < 3; ++i) __builtin_amdgcn_raw_ptr_buffer_load_lds(rU, R + (lw * 3 + i) * 1024, 16, vW[i], kblk(j) * (64 * 2752), 0, AUX_1);
    };
    if (!big && lw == 0) {
      auto rGW = __builtin_amdgcn_make_buffer_rsrc((void*)GW, 0, 1024, 0x00020000);
      __builtin_amdgcn_raw_ptr_buffer_load_lds(rGW, smem + 159744, 16, kblk(l3) * 128 + l7 * 16, 0, 0, 0);
    }
    FS(2);
    issue_w(0); issue_x(1);
    FS(3);
    auto rC = __builtin_amdgcn_make_buffer_rsrc((void*)(ks < 2 ? Cr : Ci), 0, 2048 * 2048 * 2, 0x00020000);
    auto rW = __builtin_amdgcn_make_buffer_rsrc((void*)ws, 0, WS_END, 0x00020000);
    auto rD = __builtin_amdgcn_make_buffer_rsrc((void*)Wd, 0, 1376 * 512 * 2, 0x00020000);
    int vC[4], vD[4];
#pragma unroll
    for (int i = 0; i < 4; ++i) {
      const int v = 32 * lw + 8 * i + l3;
      const int s = 32 * lw + 16 * (i & 1) + 8 * (lane >> 5) + 4 * (i >> 1) + (l3 & 3);
      vC[i] = (tm * 128 + (s ^ ((ks & 2) * 32))) * 4096 + (ks & 1) * 2048 + ((l7 ^ ((v >> 1) & 7)) * 16);
      const int r = 8 * i + l3;
      vD[i] = r * 1024 + (tn * 128 + (ks & 1) * 64) * 2 + (((l7 >> 1) ^ ((r >> 1) & 3)) * 32) + (l7 & 1) * 16;
    }
    int vL = lane * 16;
    const int sB = WS_AB + (((ks >> 1) * 2 + (tn >> 1)) * 64 + (ks & 1) * 32) * 16384 + ((lw >> 1) * 16 + (tn & 1) * 8 + ((lw ^ ks) & 1) * 4) * 1024;
    const int sHb = WS_HB + (tm * 2 + (ks >> 1)) * (43 * 4096);
    auto linear = [&](auto Nc, LAS char* R, int v, int so) LAM {
      sfor<0, decltype(Nc)::value>([&](auto Ic) LAM { __builtin_amdgcn_raw_ptr_buffer_load_lds(rW, R, 16, v, so, decltype(Ic)::value * 1024, 16); });
    };
    const int k2 = tn + 2 * ((tm >> 2) & 1), k3 = (7 * tn + ((ND * (tm & 7)) >> 3)) % ND + lw;
    auto kk = [&](int st) { return (((st >> 2) + tm) & 3) * 4 + ((st + k2) & 3); };
    auto issue_A = [&](int st) {
      LAS char* R = smem + ((st & 3) == 1 ? 9 : (st & 3) == 2 ? 6 : (st & 3) == 3 ? 1 : st == 4 ? 8 : st == 8 ? 0 : 7) * 16384 + lw * 4096;
#pragma unroll
      for (int i = 0; i < 4; ++i) __builtin_amdgcn_raw_ptr_buffer_load_lds(rC, R + i * 1024, 16, vC[i], kk(st) * 128, 0, 0);
    };
    auto issue_B = [&](int st) { linear(IC<4>{}, smem + 32768 + ((st + 3) & 3) * 16384 + lw * 4096, vL, sB + kk(st) * 32768); };
    auto issue_T = [&](int d, int part) {
      const int sl = d % 5; int kq = k3 + 4 * d; kq -= kq >= ND ? ND : 0;
      if (part & 1) linear(IC<4>{}, smem + (sl == 0 ? 7 : sl == 1 ? 6 : sl == 2 ? 1 : sl == 3 ? 8 : 9) * 16384 + lw * 4096, vL, sHb + kq * 4096);
      LAS char* R = smem + (sl ? 32768 + (sl & 3) * 16384 : 0) + lw * 4096;
#pragma unroll
      for (int i = 0; i < 4; ++i) if (part & 2) __builtin_amdgcn_raw_ptr_buffer_load_lds(rD, R + i * 1024, 16, vD[i], kq * 32768, 0, 0);
    };
    auto issue_E = [&](int e, int part) {
      const int kq = ND + 4 * ks + ((2 * e + (lw >> 1) + tn) & 3);
      if (part & 1) {
        const int so = WS_HB + ((tm * 2 + (((ks >> 1) ^ lw) & 1)) * 43 + kq) * 4096;
        sfor<0, 4>([&](auto Ic) LAM {
          constexpr int i = decltype(Ic)::value;
          __builtin_amdgcn_raw_ptr_buffer_load_lds(rW, smem + (8 + e) * 16384 + (lw & 1) * 8192 + (lw >> 1) * 1024 + i * 1024, 16, vL, so, i * 1024, 16);
        });
      }
      const int so = kq * 32768 + (lw & 1) * (128 - (ks & 1) * 256);
#pragma unroll
      for (int i = 0; i < 4; ++i) if (part & 2) __builtin_amdgcn_raw_ptr_buffer_load_lds(rD, smem + (e ? 2 : 5) * 16384 + lw * 4096 + i * 1024, 16, vD[i], so, 0, 0);
    };
    const int sel[4] = {SEL0, SEL1, SEL2, SEL3}, selh = SELH;
    u32x4 hv[6];
    u64 tag;
    unsigned tch = 0;
    constexpr int P[4] = {PA0, PA1, PA2, PA3}, S[4] = {SA0, SA1, SA2, SA3};
    auto leave = [&](auto Kc, auto Lc, auto Nc) LAM {
      constexpr int K = decltype(Kc)::value, L = decltype(Lc)::value, N = decltype(Nc)::value;
      int n = 0;
      if constexpr (!HDIR && K >= PH && L <= PH + 3 * SH) n += PH + SH * selh >= L && PH + SH * selh <= K;
      sfor<0, 4>([&](auto Gc) LAM {
        constexpr int G = decltype(Gc)::value;
        if constexpr (K >= P[G] && L <= P[G] + 4 * S[G]) n += P[G] + S[G] * sel[G] >= L && P[G] + S[G] * sel[G] <= K;
      });
      if (n == 0) wait_vm<N>(); else if (n == 1) wait_vm<N + 4>(); else if (n == 2) wait_vm<N + 8>(); else wait_vm<N + 12>();
    };
    auto fin = [&](auto Kc, auto Lc, auto Nc) LAM {
      constexpr int K = decltype(Kc)::value, L = decltype(Lc)::value, N = decltype(Nc)::value;
      if constexpr (!HDIR && K >= PH && K <= PH + 3 * SH) {
        if (K == PH + SH * selh) {
          const int sHp = WS_HB + ((m1 * 2 + (lw >> 1)) * 43 + (blk0 >> 1)) * 4096 + (lw & 1) * 2048;
#pragma unroll
          for (int t = 0; t < 6; ++t)
            if (t < 4 || big) __builtin_amdgcn_raw_buffer_store_b128(hv[t], rW, vL + (t & 1) * 1024, sHp + (t >> 1) * 4096, 16);
          sent();
        }
      }
      if constexpr (PT && K == PT)
        asm volatile("buffer_load_dword %0, %1, %2, 0 offen" : "+v"(tch) : "v"(TC1 < 0 && lane >= 32 ? OOB : (tm * 128 + lw * 32 + (lane & 31)) * 4096 + (ks & 1) * 2048 + (lane < 32 ? kk(TC0) : kk(TC1 < 0 ? 0 : TC1)) * 128), "s"(rC) : "memory");
      sfor<0, 4>([&](auto Gc) LAM {
        constexpr int G = decltype(Gc)::value;
        if constexpr (K >= P[G] && K <= P[G] + 4 * S[G]) {
          if (K == P[G] + S[G] * sel[G]) {
            constexpr int R = (ND + 7 + TWN) / TWN, r0 = (G - TWG) * R;
            if constexpr (G >= TWG && G < TWG + TWN) {
              const int t = r0 + lane;
              const int a = t < 8 ? (ND * 32 + ks * 128 + idx * 4 + (t >> 1)) * 1024 + (t & 1) * 128 : (idx * ND + t - 8) * 1024 + (ks & 1) * 128;
              asm volatile("buffer_load_dword %0, %1, %2, 0 offen" : "+v"(tch) : "v"(lane < R && t < ND + 8 ? a + lw * 256 : OOB), "s"(rD) : "memory");
            }
            issue_A(4 * G);
          }
        }
      });
      if constexpr (N >= 0) leave(Kc, Lc, Nc);
      if constexpr (!HDIR && L - 1 >= PH && L - 1 <= PH + 3 * SH && K > 16) {
        if (L - 1 == PH + SH * selh && lane == 0) __hip_atomic_store((gu64*)(ws + WS_FH) + ((m1 * 16 + n1) * 4 + lw), tag, RLX_AGENT);
      }
    };
    wait_vm<4>(); LBAR();
    if (U0E) issue_u(0);
    issue_w(1);
    const char* wse = ws;
    asm volatile("" : "+s"(wse));
    u64 epoch = *(const __attribute__((address_space(4))) u64*)(wse + WS_E);
    sfor<0, 6>([&](auto Kc) LAM {
      constexpr int k = decltype(Kc)::value;
      issue_x(k + 2); issue_w(k + 2);
      if (k == 0) asm volatile("" : "+s"(epoch));
      fin(Kc, IC<k - 1>{}, IC<9>{}); LBAR();
    });
    if (!U0E) issue_u(0);
    issue_u(1); fin(IC<6>{}, IC<5>{}, IC<U0E ? 3 : 6>{}); LBAR();
    issue_u(2); issue_u(3); issue_u(4); fin(IC<7>{}, IC<6>{}, IC<12>{}); LBAR();
    tag = epoch * 6364136223846793005ull + 1442695040888963407ull;
    const gu64* fa = (const gu64*)(ws + WS_FA) + ((((ks & 1) * 8 + l3) * 16 + (ks >> 1) * 8 + tn * 2 + ((lane >> 2) & 1)) * 4 + (lane & 3));
    u64 pv;
    fin(IC<8>{}, IC<6>{}, IC<9>{}); LBAR();
    issue_u(5); issue_u(6); fin(IC<9>{}, IC<7>{}, IC<12>{}); LBAR();
    issue_u(7); fin(IC<10>{}, IC<7>{}, IC<9>{}); LBAR();
    issue_A(1); fin(IC<11>{}, IC<10>{}, IC<4>{}); LBAR();
    asm volatile("global_load_dwordx2 %0, %1, off sc1" : "=v"(pv) : "v"(fa) : "memory");
    const bool first = (l3 >> 1) == (tm & 3);
    issue_A(2);
    asm volatile("s_waitcnt vmcnt(4)" : "+v"(pv) :: "memory");
    bool early = __all(!first || pv == tag);
    if (NPOLL > 1 && !early) {
      asm volatile("global_load_dwordx2 %0, %1, off sc1\n\ts_waitcnt vmcnt(0)" : "=v"(pv) : "v"(fa) : "memory");
      early = __all(!first || pv == tag);
    }
    if (early) issue_B(0);
    fin(IC<12>{}, IC<12>{}, IC<-1>{}); LBAR();
    issue_A(3);
    if (!early) { spin_flags(fa, first, tag); issue_B(0); }
    STAMP(1);
    issue_B(1); fin(IC<13>{}, IC<13>{}, IC<-1>{});
    if (early) leave(IC<13>{}, IC<13>{}, IC<8>{}); else leave(IC<13>{}, IC<13>{}, IC<4>{});
    LBAR();
#pragma unroll
    for (int t = 0; t < 6; ++t) hv[t] = *(LAS const u32x4*)(smem + (t < 4 ? 65536 + t * 1024 : 45056 + t * 1024) + lw * 4096 + lane * 16);
    asm volatile("global_load_dwordx2 %0, %1, off sc1" : "=v"(pv) : "v"(fa) : "memory");
    asm volatile("s_waitcnt lgkmcnt(0)" : "+v"(hv[0]), "+v"(hv[1]), "+v"(hv[2]), "+v"(hv[3]), "+v"(hv[4]), "+v"(hv[5]) :: "memory");
    issue_B(2); fin(IC<14>{}, IC<13>{}, IC<5>{}); LBAR();
    issue_B(3); leave(IC<14>{}, IC<14>{}, IC<8>{});
    asm volatile("" : "+v"(pv) :: "memory");
    if (!__all(pv == tag)) spin_flags(fa, true, tag);
    issue_B(4); fin(IC<15>{}, IC<14>{}, IC<8>{}); LBAR();
    issue_A(5); issue_B(5); fin(IC<16>{}, IC<15>{}, IC<12>{}); LBAR();
    const gu64* fh = (const gu64*)(ws + WS_FH) + (tm * 64 + lane);
    sfor<2, 12>([&](auto Jc) LAM {
      constexpr int j = decltype(Jc)::value;
      if constexpr (j == 9) asm volatile("global_load_dwordx2 %0, %1, off sc1" : "=v"(pv) : "v"(fh) : "memory");
      if constexpr (j & 3) issue_A(j + 4);
      issue_B(j + 4);
      if constexpr (j == 8) issue_T(0, 2);
      fin(IC<15 + j>{}, IC<13 + j>{}, IC<((j & 3) == 0 || (j & 3) == 1 ? 12 : 16) + (j == 9 || j == 10) + (j == 8 || j == 9 ? 4 : 0)>{});
      if constexpr (j == 11) asm volatile("" : "+v"(pv) :: "memory");
      LBAR();
    });
    asm volatile("" :: "v"(tch));
    issue_E(0, 2);
    if (!__all(pv == tag)) spin_flags(fh, true, tag);
    STAMP(2);
    sfor<1, 12>([&](auto Ic) LAM {
      constexpr int i = decltype(Ic)::value, d = i - 5;
      constexpr int DCUT[12] = {1, DSCHED}, lo = DCUT[i - 1], hi = DCUT[i];
      sfor<lo, hi>([&](auto Hc) LAM {
        constexpr int h = decltype(Hc)::value;
        if (h == 15 && lw == 3) {
          vL = OOB;
#pragma unroll
          for (int q = 0; q < 4; ++q) vD[q] = OOB;
        }
        if constexpr (h < 2) issue_E(0, 1); else if constexpr (h == 2) issue_T(0, 1); else if constexpr (h < 5) issue_E(1, h - 2);
        else issue_T((h - 3) / 2, h < HFIRST ? 1 + (h & 1) : 2 - (h & 1));
      });
      constexpr int need = i <= 3 ? i - 1 : d < 0 ? 5 : 5 + 2 * d;
      static_assert(hi <= (d <= 1 ? 2 * i + 1 : 2 * d + 9) && hi >= need, "slot free / step issued");
      if (i == 11) STAMP(3);
      wait_vm<4 * (hi - need) + (i == 1 ? 8 : 0)>();
      LBAR();
    });
    STAMP_OUT();
    if (b == 0 && wid == 7) {
#pragma unroll 1
      for (int q = 0; q < 4; ++q) spin_flags((const gu64*)(ws + WS_FA) + (q * 64 + lane) * 4, true, tag);
      if (lane == 0) __hip_atomic_store((gu64*)(ws + WS_E), tag, RLX_AGENT);
    }
  } else {
    const int w = wid;
    const char* wse = ws;
    asm volatile("" : "+s"(wse));
    const u64 epoch = *(const __attribute__((address_space(4))) u64*)(wse + WS_E);
    const u64 tag = epoch * 6364136223846793005ull + 1442695040888963407ull;
    auto rW = __builtin_amdgcn_make_buffer_rsrc((void*)ws, 0, WS_END, 0x00020000);
    const unsigned lds0 = (unsigned)(size_t)smem;
    auto rd128 = [&](unsigned a, int o) { return *(LAS const bf16x8*)((LAS char*)(size_t)a + o); };
    auto rdtr = [&](unsigned a, int o, int d) {
      bf16x4 lo = __builtin_amdgcn_ds_read_tr16_b64_v4bf16((LAS bf16x4*)((LAS char*)(size_t)a + o));
      bf16x4 hi = __builtin_amdgcn_ds_read_tr16_b64_v4bf16((LAS bf16x4*)((LAS char*)(size_t)a + o + d));
      return __builtin_shufflevector(lo, hi, 0, 1, 2, 3, 4, 5, 6, 7);
    };
    const int wm = w >> 1, wn = w & 1;
    unsigned aX[2], aD0[2];
    const unsigned vl = opq(lane * 16);
    aD0[0] = opq(lds0 + (lane >> 2) * 128 + (((2 * wn) ^ ((lane >> 3) & 3)) * 32) + (lane & 3) * 8 + 32768);
    int sY = WS_YP + wgl * 24576 + w * 2048;
    asm volatile("" : "+s"(sY));
    auto stage1 = [&](auto NBc) LAM {
      constexpr int NB = decltype(NBc)::value, NF = NB == 4 ? 9 : 10, D = 4;
      const int i16 = lane & 15, g = lane >> 4;
      unsigned aH[2], aW[2];
#pragma unroll
      for (int s = 0; s < 2; ++s) {
        aH[s] = opq(lds0 + i16 * 128 + (((4 * s + g) ^ ((i16 >> 1) & 7)) * 16)); aX[s] = opq(aH[s] + w * 4096);
        aW[s] = opq(lds0 + (8 * g + (i16 >> 2)) * 192 + ((s ^ (g & 1)) * 32) + (lane & 3) * 8 + wslot(0));
      }
      const unsigned aG = opq(lds0 + g * 16 + 159744);
      bf16x8 xf[16][2], fr[8];
      f32x4 G[NB][2];
      WAIT_LGKM0();
      BARRIER();
      STAMP(1);
      {
        f32x4 ab[2][4], z[2];
        auto rdF = [&](auto Fc) LAM {
          constexpr int F = decltype(Fc)::value, kq = F / NF, j = kq >> 1, s = kq & 1, f = F % NF;
          if (j && !s && !f) {
            constexpr int dl = j % 3 ? SLOT : -2 * SLOT;
#pragma unroll
            for (int t = 0; t < 2; ++t) { aX[t] = opq(aX[t] + dl); aH[t] = opq(aH[t] + dl); aW[t] = opq(aW[t] + (wslot(j) - wslot(j - 1))); }
          }
          if (f == 0) { xf[kq][0] = rd128(aX[s], 20480); xf[kq][1] = rd128(aX[s], 22528); }
          if constexpr (f < 4) fr[F & 7] = rd128(aH[s], f * 2048);
          else if constexpr (f < 4 + NB) fr[F & 7] = rdtr(aW[f & 1], ((f - 4) >> 1) * 64 + s * 6144, 768);
          else fr[F & 7] = rd128(aG, kq * 64);
        };
        auto mm = [&](auto Fc, int mb) LAM {
          constexpr int F = decltype(Fc)::value, kq = F / NF, f = F % NF;
          if constexpr (f < 4) mfx<kq == 0>(ab[mb][f], xf[kq][mb], fr[F & 7]);
          else if constexpr (f < 4 + NB) mfx<kq == 0>(G[f - 4][mb], fr[F & 7], xf[kq][mb]);
          else mfx<kq == 0>(z[mb], fr[F & 7], xf[kq][mb]);
        };
        sfor<0, D>(rdF);
        SB();
        sfor<0, 16 * NF>([&](auto Fc) LAM {
          constexpr int F = decltype(Fc)::value;
          if constexpr (F + D < 16 * NF && ((F + D) % (2 * NF)) == 0) { BARRIER(); SB(); }
          mm(Fc, 0); SB();
          if constexpr (F + D < 16 * NF) rdF(IC<F + D>{});
          SB();
          mm(Fc, 1); SB();
        });
        WAIT_LGKM0(); BARRIER();
        settle();
#pragma unroll
        for (int c = 0; c < 4; ++c) { pin(ab[0][c]); pin(ab[1][c]); }
        if constexpr (NB == 4) { pin(z[0]); pin(z[1]); }
        STAMP(2);
        const unsigned sgn = n1 < 8 ? 0u : 0x80008000u;
        const int sA = WS_AB + (((n1 >> 2) * 64 + m1 * 4 + w) * 16 + (n1 & 3) * 4) * 1024;
#pragma unroll
        for (int c = 0; c < 4; ++c) {
          u32x4 v = {cvtpk(ab[0][c][0], ab[0][c][1]) ^ sgn, cvtpk(ab[0][c][2], ab[0][c][3]) ^ sgn,
                     cvtpk(ab[1][c][0], ab[1][c][1]) ^ sgn, cvtpk(ab[1][c][2], ab[1][c][3]) ^ sgn};
          SB();
          __builtin_amdgcn_raw_buffer_store_b128(v, rW, vl + c * 1024, sA, 16);
          sent();
        }
        if (NB == 4 && n1 == 3 && lane < 16) {
#pragma unroll
          for (int mb = 0; mb < 2; ++mb) {
            float gv = __builtin_amdgcn_rcpf(1.0f + __builtin_amdgcn_exp2f(-1.4426950409f * z[mb][0]));
            __hip_atomic_store((gu32*)(ws + WS_G) + m1 * 128 + w * 32 + lane * 2 + mb, __builtin_bit_cast(unsigned, gv), RLX_AGENT);
          }
        }
        SB();
      }
      STAMP(3);
      {
        gu64* const fa = (gu64*)(ws + WS_FA) + ((m1 * 16 + n1) * 4 + w);
        f32x4 U[NB][2];
        float t, r;
        auto rdF = [&](auto Fc) LAM {
          constexpr int F = decltype(Fc)::value, kq = F / NB, j = kq >> 1, s = kq & 1, f = F % NB;
          if ((j == 0 || j == 1 || j == 4) && !s && !f) {
            constexpr int dl = j == 0 ? 147456 - wslot(7) : -73728;
#pragma unroll
            for (int c = 0; c < 2; ++c) aW[c] = opq(aW[c] + dl);
          }
          if (kq >= 14 && f == 0) { xf[kq][0] = rd128(aX[s], 20480); xf[kq][1] = rd128(aX[s], 22528); }
          fr[F & 7] = rdtr(aW[f & 1], (j == 0 ? 0 : j < 4 ? j - 1 : j - 4) * 12288 + (f >> 1) * 64 + s * 6144, 768);
        };
        auto act = [&](auto Fc) LAM {
          constexpr int F = decltype(Fc)::value, v = F >> 2, ph = F & 3;
          if constexpr (ph == 0) asm volatile("v_fmaak_f32 %0, %1, %1, 0x41b2e92f\n\tv_mul_f32 %0, %1, %0" : "=&v"(t) : "v"(G[v >> 2][0][v & 3]));
          if constexpr (ph == 1) {
            asm volatile("v_mul_f32 %0, 0xbdd2d3e8, %0" : "+v"(t));
            if constexpr (v > 0) asm volatile("v_mul_f32 %0, %0, %1" : "+v"(G[(v - 1) >> 2][0][(v - 1) & 3]) : "v"(r));
          }
          if constexpr (ph == 2) asm volatile("v_exp_f32 %0, %0" : "+v"(t));
          if constexpr (ph == 3) asm volatile("v_add_f32 %1, 1.0, %1\n\tv_rcp_f32 %0, %1" : "=v"(r), "+v"(t));
        };
        sfor<0, D>(rdF);
        SB();
        sfor<0, 16 * NB>([&](auto Fc) LAM {
          constexpr int F = decltype(Fc)::value, f = F % NB;
          if (F + D == FA_KQ * NB) { wait_vm<0>(); if (lane == 0) __hip_atomic_store(fa, tag, RLX_AGENT); SB(); }
          if constexpr (((F + D) % (2 * NB)) == 0 && ((0x2e >> ((F + D) / (2 * NB))) & 1)) { BARRIER(); SB(); }
          mfx<F / NB == 0>(U[f][0], fr[F & 7], xf[F / NB][0]); SB();
          if constexpr (F + D < 16 * NB) rdF(IC<F + D>{});
          SB();
          mfx<F / NB == 0>(U[f][1], fr[F & 7], xf[F / NB][1]); SB();
          act(Fc); SB();
        });
        WAIT_LGKM0(); BARRIER();
        settle();
        asm volatile("v_mul_f32 %0, %0, %1" : "+v"(G[NB - 1][0][3]) : "v"(r));
#pragma unroll
        for (int c = 0; c < NB; ++c) { pin(U[c][0]); pin(U[c][1]); }
        STAMP(4);
        XS(8);
        LAS char* const hs = (LAS char*)(size_t)opq(lds0 + w * 4096 + 49152 + vl);
        f32x2 c1 = {-2.302208198f, -2.302208198f}, c2 = {-0.1029432397f, -0.1029432397f};
        asm volatile("" : "+v"(c1), "+v"(c2));
        u32x4 hp[NB / 2][2];
        auto half0 = [&](int i) {
          const f32x4 h0 = G[2 * i][0] * U[2 * i][0], h1 = G[2 * i + 1][0] * U[2 * i + 1][0];
          hp[i][0] = u32x4{cvtpk(h0[0], h0[1]), cvtpk(h0[2], h0[3]), cvtpk(h1[0], h1[1]), cvtpk(h1[2], h1[3])};
          SB();
        };
        auto half1 = [&](int i) {
          const f32x4 g0 = G[2 * i][1], u0 = U[2 * i][1], g1 = G[2 * i + 1][1], u1 = U[2 * i + 1][1];
          hp[i][1] = u32x4{geglu(g0.xy, u0.xy, c1, c2), geglu(g0.zw, u0.zw, c1, c2), geglu(g1.xy, u1.xy, c1, c2), geglu(g1.zw, u1.zw, c1, c2)};
          SB();
        };
        const int sHp = WS_HB + ((m1 * 2 + (w >> 1)) * 43 + (blk0 >> 1)) * 4096 + (w & 1) * 2048;
        auto put = [&](int i, int mb) {
          if (HDIR) __builtin_amdgcn_raw_buffer_store_b128(hp[i][mb], rW, vl + mb * 1024, sHp + i * 4096, 16);
          else *(LAS u32x4*)(hs + (i < 2 ? 16384 + i * 2048 : 0) + mb * 1024) = hp[i][mb];
          if (HDIR) sent(); else SB();
        };
#pragma unroll
        for (int i = 0; i < NB / 2; ++i) {
          if (HORD == 0) { half0(i); put(i, 0); half1(i); put(i, 1); }
          if (HORD == 1) { half0(i); half1(i); }
          if (HORD == 2) { half1(i); put(i, 1); half0(i); if (i) put(i - 1, 0); }
          XS(9 + i);
        }
        if (HORD == 1) for (int i = 0; i < NB; ++i) put(i >> 1, i & 1);
        if (HORD == 2) put(NB / 2 - 1, 0);
        XS(13);
        WAIT_LGKM0();
        XS(14);
        SB();
      }
    };
    if (big) stage1(IC<6>{}); else stage1(IC<4>{});
    STAMP(5);
    unsigned aH0, aH1, aD1[2];
    bf16x8 hf[4][2], df[4][2];
    auto rh = [&](int sl, int j, int mb) { hf[j][mb] = rd128(sl == 2 ? aH0 : aH1, (sl == 1 ? 0 : sl < 3 ? 1 : sl - 1) * 16384 + j * 4096 + mb * 1024); };
    auto rd = [&](int sl, int j, int c) { df[j][c] = rdtr(sl == 0 ? aD1[c] : aD0[c], (sl & 3) * 16384 + j * 4096, 2048); };
    f32x4 own[2][2];
    u32x4 pick[2];
    int vO, sZ[3];
    gu64* fy; const gu64* fz;
    float sc[64];
    f32x4 ac2[4][4];
    float gn[4];
    f32x2 gv[2];
    {
      unsigned aA[2], aL[2], aZ[2];
#pragma unroll
      for (int s = 0; s < 2; ++s) aZ[s] = opq(aX[s] + (114688 - SLOT + (wm - w) * 4096));
      const unsigned aB = opq(vl + (lds0 + wn * 2048 + 32768));
      f32x4 acc[4][4];
      bf16x8 fa[2][4], fb[2][4];
      auto rdA = [&](int set, int mb, int slot, int s) { fa[set][mb] = rd128(slot == 0 ? aZ[s] : slot == 3 ? aL[s] : aA[s], (slot == 1 ? 49152 : slot == 3 ? 16384 : 0) + (mb >> 1) * 8192 + (mb & 1) * 2048); };
      auto rdB = [&](int set, int nb, int slot, int s) { fb[set][nb] = rd128(aB, ((slot + 3) & 3) * 16384 + s * 8192 + (nb >> 1) * 4096 + (nb & 1) * 1024); };
      auto half = [&](int u, int s) LAM {
        const int cur = s, nxt = s ^ 1, ns = s ^ 1, nslot = s ? ((u + 1) & 3) : u;
        auto mm = [&](int nb, int mb) LAM { mfa(acc[nb][mb], fb[cur][nb], fa[cur][mb]); SB(); };
        if (s == 1) { WAIT_LGKM0(); BARRIER(); SB(); }
        mm(0, 0); rdA(nxt, 0, nslot, ns); SB();
        mm(1, 0); rdB(nxt, 0, nslot, ns); SB();
        mm(2, 0); rdB(nxt, 1, nslot, ns); SB();
        mm(3, 0); rdB(nxt, 2, nslot, ns); SB();
        mm(0, 1); rdB(nxt, 3, nslot, ns); SB();
        mm(1, 1); rdA(nxt, 1, nslot, ns); SB();
        mm(2, 1); rdA(nxt, 2, nslot, ns); SB();
        mm(3, 1); rdA(nxt, 3, nslot, ns); SB();
        mm(0, 2); mm(1, 2); mm(2, 2); mm(3, 2);
        mm(0, 3); mm(1, 3); mm(2, 3); mm(3, 3);
      };
      XS(15);
      BARRIER();
      STAMP(6);
      rdA(0, 0, 0, 0);
#pragma unroll
      for (int i = 0; i < 4; ++i) rdB(0, i, 0, 0);
#pragma unroll
      for (int i = 1; i < 4; ++i) rdA(0, i, 0, 0);
      SB();
#pragma unroll
      for (int s = 0; s < 2; ++s) { aL[s] = opq(aZ[s] - 114688); aA[s] = opq(aZ[s] - 16384); }
      aH0 = opq(vl + (lds0 + wm * 2048)); aH1 = opq(aH0 + 98304);
      aD0[1] = opq(aD0[0] ^ 32); aD1[0] = opq(aD0[0] - 32768); aD1[1] = opq(aD0[1] - 32768);
#pragma unroll
      for (int i = 0; i < 16; ++i) { acc[i >> 2][i & 3] = f32x4{}; pin(acc[i >> 2][i & 3]); }
      bf16x4 fw[8];
      asm volatile("" : "=v"(gv[0]), "=v"(gv[1]), "=v"(fw[0]), "=v"(fw[1]), "=v"(fw[2]), "=v"(fw[3]), "=v"(fw[4]), "=v"(fw[5]), "=v"(fw[6]), "=v"(fw[7]));
      const unsigned aE = opq(aH0 + wm * 2048 + 131072), vG = opq((vl & 240) >> 1);
      {
        const int lane = vl >> 4, g = lane >> 4, q = (lane & 15) >> 2, p = lane & 3;
        const unsigned one = g == q ? 0x3f80u << (16 * (p & 1)) : 0u, o0 = p < 2 ? one : 0u, o1 = one - o0;
        pick[0] = u32x4{o0, o1, 0u, 0u}; pick[1] = u32x4{0u, 0u, o0, o1};
        vO = ((tm * 128 + (ks >> 1) * 64 + wm * 32 + 8 * q + p) * 512 + tn * 128 + (ks & 1) * 64 + wn * 32 + (g & 1) * 16 + (g >> 1) * 8) * 2;
        fy = (gu64*)(ws + WS_FY) + (wgl * 4 + w) * 8;
        fz = (const gu64*)(ws + WS_FY) + ((((xcd & 4) | p) * 32 + idx) * 4 + w) * 8;
#pragma unroll
        for (int o = 0; o < 3; ++o) sZ[o] = WS_YP + (((xcd ^ (o + 1)) * 32 + idx) * 3 + o) * 8192 + w * 2048;
        asm volatile("" : "+v"(pick[0]), "+v"(pick[1]), "+v"(vO), "+s"(fy), "+v"(fz), "+s"(sZ[0]), "+s"(sZ[1]), "+s"(sZ[2]));
      }
      SB();
#pragma unroll 1
      for (int it = 0; it < 4; ++it) {
        if (HDIR && it == FH_IT) { wait_vm<0>(); if (vl == 0) __hip_atomic_store((gu64*)(ws + WS_FH) + ((m1 * 16 + n1) * 4 + w), tag, RLX_AGENT); SB(); }
        half(0, 0); half(0, 1); half(1, 0); half(1, 1);
        { const unsigned z = it == 0 ? 131072 : it == 1 ? 0 : 114688; aZ[0] = opq(aL[0] + z); aZ[1] = opq(aL[1] + z); SB(); }
        if (it == 3) {
          const int sG = WS_G + (tm * 128 + (ks >> 1) * 64 + wm * 32) * 4;
          asm volatile("buffer_load_dwordx2 %0, %3, %4, %5 offen sc1\n\tbuffer_load_dwordx2 %1, %3, %4, %6 offen sc1\n\tv_mov_b32 %2, %7"
                       : "+v"(gv[0]), "+v"(gv[1]), "+v"(aZ[0]) : "v"(vG), "s"(rW), "s"(sG), "s"(sG ^ 256), "v"(aE) : "memory");
          SB();
        }
        half(2, 0); half(2, 1);
        if (it == 3) {
#pragma unroll
          for (int i = 0; i < 8; ++i)
            asm volatile("ds_read_b64_tr_b16 %0, %1 offset:%2" : "+v"(fw[i]) : "v"(aD0[(i >> 1) & 1]), "n"(49152 + (i >> 2) * 4096 + (i & 1) * 2048) : "memory");
          SB();
        }
        half(3, 0); half(3, 1);
      }
      STAMP(7);
      XS(0);
      auto rdE = [&](int set, int mb, int e, int s) { fa[set][mb] = rd128(aE, e * 16384 + s * 1024 + (mb >> 1) * 8192 + (mb & 1) * 2048); };
      auto rdW = [&](int set, int nb, int e, int s) { fb[set][nb] = rdtr(aD0[nb & 1], (e ? 0 : 49152) + s * 8192 + (nb >> 1) * 4096, 2048); };
#pragma unroll
      for (int i = 0; i < 4; ++i) fb[0][i] = __builtin_shufflevector(fw[2 * i], fw[2 * i + 1], 0, 1, 2, 3, 4, 5, 6, 7);
      SB();
      XS(1);
      sfor<0, 3>([&](auto Hc) LAM {
        constexpr int H = decltype(Hc)::value, cur = H & 1, nxt = cur ^ 1, e = (H + 1) >> 1;
        auto mm = [&](int nb, int mb) LAM { mfx<H == 0>(ac2[nb][mb], fb[cur][nb], fa[cur][mb]); SB(); };
        auto ra = [&](int i) LAM { rdE(nxt, i, e, nxt); SB(); };
        auto rb = [&](int i) LAM { rdW(nxt, i, e, nxt); SB(); };
        auto gs = [&](int i, int c) LAM {
#pragma unroll
          for (int n = H * 20 - 8 + i; n < H * 20 - 8 + i + c; ++n) {
            const int k = n + (n < 8 ? 8 : 16);
            if (n >= 0 && n < 48) asm volatile("v_mul_f32 %0, %1, %0" : "+v"(sc[k]) : "v"(gv[(k >> 3) & 1][(k >> 2) & 1]));
            if (n >= 48) asm volatile("v_sub_f32 %0, 1.0, %1" : "=v"(gn[n - 48]) : "v"(gv[(n >> 1) & 1][n & 1]));
          }
          SB();
        };
        if (cur == 1) { WAIT_LGKM0(); BARRIER(); SB(); }
        mm(0, 0); ra(0); gs(0, 1);
        mm(1, 0); rb(0);
        mm(2, 0); rb(1);
        mm(3, 0); rb(2);
        mm(0, 1); rb(3);
        mm(1, 1); ra(1); gs(1, 1);
        mm(2, 1); ra(2); gs(2, 1);
        mm(3, 1); ra(3); gs(3, 1);
        mm(0, 2); gs(4, 2);
        mm(1, 2); gs(6, 2);
        if (H == 0) {
          asm volatile("s_waitcnt vmcnt(0)" : "+v"(gv[0]), "+v"(gv[1]) :: "memory");
#pragma unroll
          for (int i = 0; i < 16; ++i) pin(acc[i >> 2][i & 3]);
#pragma unroll
          for (int i = 0; i < 64; ++i) sc[i] = acc[i >> 4][(i >> 2) & 3][i & 3];
          SB();
        }
        mm(2, 2); gs(8, 2);
        mm(3, 2); gs(10, 2);
        mm(0, 3); gs(12, 2);
        mm(1, 3); gs(14, 2);
        mm(2, 3); gs(16, 2);
        mm(3, 3); gs(18, 2);
      });
      WAIT_LGKM0(); BARRIER(); SB();
      XS(2);
      u32x4 yq[6];
      sfor<0, 18>([&](auto Mc) LAM {
        constexpr int M = decltype(Mc)::value, o = ((M >> 2) + 1) & 3, C = M - 6, oc = (C >> 2) + 1;
        if constexpr (M < 16) { mfa(ac2[(o & 1) * 2 + (M & 1)][(o & 2) + ((M >> 1) & 1)], fb[1][(o & 1) * 2 + (M & 1)], fa[1][(o & 2) + ((M >> 1) & 1)]); SB(); }
        if constexpr (M == 0 || M == 2 || M == 3 || M == 4) rd(0, M > 2, M & 1 ^ (M >= 2));
        if constexpr (M == 1 || M == 5) { rh(0, M >> 2, 0); rh(0, M >> 2, 1); }
        if constexpr (C >= 0) {
          constexpr int nb = (oc & 1) * 2 + (C & 1), mb = (oc & 2) + ((C >> 1) & 1), k = nb * 16 + mb * 4;
          asm volatile("v_fmac_f32 %2, %6, %7\n\tv_fmac_f32 %3, %6, %8\n\tv_fmac_f32 %4, %6, %9\n\tv_fmac_f32 %5, %6, %10\n\t"
                       "v_cvt_pk_bf16_f32 %0, %2, %3\n\tv_cvt_pk_bf16_f32 %1, %4, %5"
                       : "=&v"(yq[C >> 1][(C & 1) * 2]), "=v"(yq[C >> 1][(C & 1) * 2 + 1]), "+v"(sc[k]), "+v"(sc[k + 1]), "+v"(sc[k + 2]), "+v"(sc[k + 3])
                       : "v"(gn[mb]), "v"(ac2[nb][mb][0]), "v"(ac2[nb][mb][1]), "v"(ac2[nb][mb][2]), "v"(ac2[nb][mb][3]));
          if constexpr (C & 1) { SB(); __builtin_amdgcn_raw_buffer_store_b128(yq[C >> 1], rW, vl + ((C >> 1) & 1) * 1024, sY + (C >> 2) * 8192, 16); sent(); }
        }
        SB();
      });
    }
    STAMP(8);
    XS(3);
    {
      const int lane = vl >> 4;
      f32x4 acc[2][2];
      u32x4 y[3][2];
      const bool fzl = (lane & 3) != ks;
      u64 pv; bool late = true;
      auto rO = __builtin_amdgcn_make_buffer_rsrc((void*)out, 0, 2048 * 512 * 2, 0x00020000);
      asm volatile("" : "+s"(rO));
      auto load_y = [&]() {
#pragma unroll
        for (int o = 0; o < 3; ++o) {
#pragma unroll
          for (int mb = 0; mb < 2; ++mb)
            asm volatile("buffer_load_dwordx4 %0, %1, %2, %3 offen offset:%4 sc1" : "=v"(y[o][mb]) : "v"(vl), "s"(rW), "s"(sZ[o]), "n"(mb * 1024) : "memory");
        }
      };
      auto kstep = [&](auto Fc, int j, int sl, int jn) LAM {
        constexpr bool F = decltype(Fc)::value;
        mfx<F>(acc[0][0], df[j][0], hf[j][0]); SB(); if (jn >= 0) rd(sl, jn, 0); SB();
        mfx<F>(acc[1][0], df[j][1], hf[j][0]); SB(); if (jn >= 0) { rh(sl, jn, 0); rh(sl, jn, 1); } SB();
        mfx<F>(acc[0][1], df[j][0], hf[j][1]); SB(); if (jn >= 0) rd(sl, jn, 1); SB();
        mfx<F>(acc[1][1], df[j][1], hf[j][1]); SB();
      };
      auto check = [&]() LAM {
        asm volatile("s_waitcnt vmcnt(0)" : "+v"(pv) :: "memory");
        late = !__all(!fzl || pv == tag);
        if (!late) load_y();
        SB();
      };
      auto put = [&](auto Pc) LAM {
        constexpr int P = decltype(Pc)::value - OWNP;
        if constexpr (P >= 0 && P < 16) {
          constexpr int nb = (P >> 2) & 1, mb = (P >> 1) & 1, e = (P & 1) * 2, k = nb * 16 + mb * 4 + e;
          if constexpr (P < 8) asm volatile("v_mul_f32 %0, %2, %0\n\tv_mul_f32 %1, %2, %1" : "+v"(sc[k]), "+v"(sc[k + 1]) : "v"(gv[0][mb]));
          else asm volatile("v_fmac_f32 %0, %2, %3\n\tv_fmac_f32 %1, %2, %4" : "+v"(sc[k]), "+v"(sc[k + 1]) : "v"(gn[mb]), "v"(ac2[nb][mb][e]), "v"(ac2[nb][mb][e + 1]));
          if constexpr (P >= 8 && e) own[nb][mb] = f32x4{sc[k - 2], sc[k - 1], sc[k], sc[k + 1]};
          SB();
        }
        if constexpr (decltype(Pc)::value == FYK) { wait_vm<0>(); if (lane == 0) __hip_atomic_store(fy, tag, RLX_AGENT); SB(); }
        if constexpr (decltype(Pc)::value == FYP) { asm volatile("global_load_dwordx2 %0, %1, off sc1" : "=v"(pv) : "v"(fz) : "memory"); SB(); }
        if constexpr (decltype(Pc)::value == FYC) check();
      };
      auto sum_y = [&]() LAM {
        asm volatile("s_waitcnt vmcnt(0)" : "+v"(y[0][0]), "+v"(y[0][1]), "+v"(y[1][0]), "+v"(y[1][1]), "+v"(y[2][0]), "+v"(y[2][1]) :: "memory");
#pragma unroll
        for (int i = 0; i < 12; ++i) mfa(own[i & 1][(i >> 1) & 1], __builtin_bit_cast(bf16x8, pick[i & 1]), __builtin_bit_cast(bf16x8, y[i >> 2][(i >> 1) & 1]));
        settle();
#pragma unroll
        for (int i = 0; i < 4; ++i) pin(own[i & 1][i >> 1]);
        SB();
      };
      static_assert(FYK < FYP && FYP < FYC && FYC < ND, "stores, flag, sample, check");
      sfor<0, 6>([&](auto Dc) LAM {
        constexpr int d = decltype(Dc)::value, sl = d % 5;
        kstep(IC<d == 0>{}, 0, sl, 2); put(IC<4 * d>{}); kstep(IC<0>{}, 1, sl, 3); put(IC<4 * d + 1>{});
        BARRIER(); SB();
        kstep(IC<0>{}, 2, (sl + 1) % 5, 0); put(IC<4 * d + 2>{}); kstep(IC<0>{}, 3, (sl + 1) % 5, 1); put(IC<4 * d + 3>{});
      });
      kstep(IC<0>{}, 0, 1, 2); put(IC<24>{}); kstep(IC<0>{}, 1, 1, -1); put(IC<25>{}); kstep(IC<0>{}, 2, 1, -1); put(IC<26>{});
      settle();
#pragma unroll
      for (int i = 0; i < 4; ++i) { pin(acc[i >> 1][i & 1]); own[i >> 1][i & 1] += gn[i & 1] * acc[i >> 1][i & 1]; pin(own[i >> 1][i & 1]); }
      SB();
      STAMP(9);
      if (late) {
        if (PSPIN) {
          u64 p0, p1, p2;
          asm volatile("global_load_dwordx2 %0, %2, off sc1\n\ts_sleep %3\n\tglobal_load_dwordx2 %1, %2, off sc1" : "=&v"(p0), "=&v"(p1) : "v"(fz), "n"(PSPIN) : "memory");
#pragma unroll 1
          for (int s = 0; s < SPIN_CAP; ++s) {
            asm volatile("s_sleep %3\n\tglobal_load_dwordx2 %0, %2, off sc1\n\ts_waitcnt vmcnt(2)" : "=&v"(p2), "+v"(p0) : "v"(fz), "n"(PSPIN) : "memory");
            if (__all(!fzl || p0 == tag)) break;
            asm volatile("s_sleep %3\n\tglobal_load_dwordx2 %0, %2, off sc1\n\ts_waitcnt vmcnt(2)" : "=&v"(p0), "+v"(p1) : "v"(fz), "n"(PSPIN) : "memory");
            if (__all(!fzl || p1 == tag)) break;
            asm volatile("s_sleep %3\n\tglobal_load_dwordx2 %0, %2, off sc1\n\ts_waitcnt vmcnt(2)" : "=&v"(p1), "+v"(p2) : "v"(fz), "n"(PSPIN) : "memory");
            if (__all(!fzl || p2 == tag)) break;
          }
          load_y();
          asm volatile("s_waitcnt vmcnt(6)" : "+v"(p0), "+v"(p1), "+v"(p2) :: "memory");
        } else { spin_flags(fz, fzl, tag); load_y(); }
      }
      sum_y();
      STAMP(10);
#pragma unroll
      for (int mb = 0; mb < 2; ++mb) {
        const f32x4 r0 = own[0][mb], r1 = own[1][mb];
        auto s0 = __builtin_amdgcn_permlane16_swap(cvtpk(r0[0], r0[1]), cvtpk(r1[0], r1[1]), false, false);
        auto s1 = __builtin_amdgcn_permlane16_swap(cvtpk(r0[2], r0[3]), cvtpk(r1[2], r1[3]), false, false);
        u32x4 v = {s0[0], s1[0], s0[1], s1[1]};
        SB();
        __builtin_amdgcn_raw_buffer_store_b128(v, rO, vO, mb * 4096, AUX_O);
        sent();
      }
      STAMP(11);
      STAMP_OUT();
    }
  }
#endif
}

extern "C" void kernel_launch(void* const* d_in, const int* in_sizes, int n_in, void* d_out, int out_size, void* d_ws,
                              size_t ws_size, hipStream_t stream) {
  (void)in_sizes; (void)n_in; (void)out_size; (void)ws_size;
  constexpr int kLds = 163840;
  (void)hipFuncSetAttribute((const void*)fused_kernel, hipFuncAttributeMaxDynamicSharedMemorySize, kLds);
  fused_kernel<<<256, 512, kLds, stream>>>((const u16*)d_in[0], (const u16*)d_in[1], (const u16*)d_in[2], (const u16*)d_in[3],
                                           (const u16*)d_in[4], (const u16*)d_in[6], (const u16*)d_in[7], (const u16*)d_in[8],
                                           (const u16*)d_in[9], (u16*)d_out, (char*)d_ws + WSOFF);
}
```

```cpp
#include <hip/hip_runtime.h>
#include <hip/hip_bf16.h>

typedef unsigned short u16;
typedef unsigned long long u64;
typedef __bf16 bf16x8 __attribute__((ext_vector_type(8)));
typedef __bf16 bf16x4 __attribute__((ext_vector_type(4)));
typedef __bf16 bf16x2 __attribute__((ext_vector_type(2)));
typedef float f32x4 __attribute__((ext_vector_type(4)));
typedef float f32x2 __attribute__((ext_vector_type(2)));
typedef unsigned u32x4 __attribute__((ext_vector_type(4)));
typedef unsigned u32x2 __attribute__((ext_vector_type(2)));
typedef __attribute__((address_space(1))) u64 gu64;
typedef __attribute__((address_space(1))) unsigned gu32;
#define LAS __attribute__((address_space(3)))
#define DI __device__ __forceinline__
#define SB() __builtin_amdgcn_sched_barrier(0)
#define BARRIER() do { asm volatile("" ::: "memory"); __builtin_amdgcn_s_barrier(); asm volatile("" ::: "memory"); } while (0)
#define WAIT_LGKM0() __builtin_amdgcn_s_waitcnt(0xC07F)
#define RLX_AGENT __ATOMIC_RELAXED, __HIP_MEMORY_SCOPE_AGENT
template <int V> struct IC { static constexpr int value = V; };
template <int I, int N, class Fn> DI void sfor(Fn&& fn) { if constexpr (I < N) { fn(IC<I>{}); sfor<I + 1, N>(fn); } }
#define LAM __attribute__((always_inline))
template <int N> DI void wait_vm() { asm volatile("s_waitcnt vmcnt(%0)" :: "n"(N) : "memory"); }

constexpr int FA_KQ = 4;
constexpr int PH = 14;
constexpr int FYK = 5, FYP = 14, FYC = 23;
#define DSCHED 2, 5, 7, 9, 11, 11, 13, 15, 17, 17, 17

constexpr int WS_E = 0, WS_FA = 4096, WS_FH = 12288, WS_G = 20480, WS_FY = 65536;
constexpr int WS_AB = 1 << 20, WS_HB = 5 << 20, WS_YP = 11 << 20, WS_END = 17 << 20;
constexpr int SPIN_CAP = 1 << 20;
constexpr int ND = 27;

DI unsigned cvtpk(float lo, float hi) { f32x2 v = {lo, hi}; bf16x2 b = __builtin_convertvector(v, bf16x2); return __builtin_bit_cast(unsigned, b); }
DI float bflo(unsigned v) { return __builtin_bit_cast(float, v << 16); }
DI float bfhi(unsigned v) { return __builtin_bit_cast(float, v & 0xffff0000u); }
DI unsigned geglu(f32x2 x, f32x2 u, f32x2 c1, f32x2 c2) {
  f32x2 z = x * __builtin_elementwise_fma(x * x, c2, c1);
  f32x2 e = {__builtin_amdgcn_exp2f(z[0]), __builtin_amdgcn_exp2f(z[1])};
  e += 1.0f;
  f32x2 r = {__builtin_amdgcn_rcpf(e[0]), __builtin_amdgcn_rcpf(e[1])};
  r *= x * u;
  return cvtpk(r[0], r[1]);
}
DI void mfa(f32x4& c, bf16x8 a, bf16x8 b) { asm volatile("v_mfma_f32_16x16x32_bf16 %0, %1, %2, %0" : "+v"(c) : "v"(a), "v"(b)); }
DI void mf0(f32x4& c, bf16x8 a, bf16x8 b) { asm volatile("v_mfma_f32_16x16x32_bf16 %0, %1, %2, 0" : "=v"(c) : "v"(a), "v"(b)); }
template <bool FIRST> DI void mfx(f32x4& c, bf16x8 a, bf16x8 b) { if constexpr (FIRST) mf0(c, a, b); else mfa(c, a, b); }
DI void settle() { asm volatile("s_nop 7"); }
DI void sent() { SB(); asm volatile("s_nop 1"); SB(); }
DI void pin(f32x4& c) { asm volatile("" : "+v"(c)); }
DI unsigned opq(unsigned a) { asm volatile("" : "+v"(a)); return a; }
DI int lane_id() { int l; asm volatile("v_mbcnt_lo_u32_b32 %0, -1, 0\n\tv_mbcnt_hi_u32_b32 %0, -1, %0" : "=v"(l)); return l; }
DI u64 uni64(u64 v) {
  unsigned lo = __builtin_amdgcn_readfirstlane((unsigned)v), hi = __builtin_amdgcn_readfirstlane((unsigned)(v >> 32));
  return ((u64)hi << 32) | lo;
}
constexpr int SLOT = 36864;
constexpr int wslot(int j) { return (j & 3) == 0 ? 2 * SLOT + 8192 : (j & 3) == 1 ? 8192 : (j & 3) == 2 ? SLOT + 8192 : 131072; }
DI void spin_flags(const gu64* p, bool active, u64 tag) {
#pragma unroll 1
  for (int s = 0; s < SPIN_CAP; ++s) {
    u64 v = tag;
    if (active) v = __hip_atomic_load(p, RLX_AGENT);
    if (__all(v == tag)) break;
    __builtin_amdgcn_s_sleep(1);
  }
}

__global__ __launch_bounds__(512, 2) void fused_kernel(
    const u16* __restrict__ X, const u16* __restrict__ Cr, const u16* __restrict__ Ci, const u16* __restrict__ Hr,
    const u16* __restrict__ Hi, const u16* __restrict__ Wg, const u16* __restrict__ Wu, const u16* __restrict__ Wd,
    const u16* __restrict__ GW, u16* __restrict__ out, char* __restrict__ ws) {
#if defined(__HIP_DEVICE_COMPILE__)
  extern __shared__ __attribute__((aligned(16))) char smem_[];
  LAS char* smem = (LAS char*)smem_;
  const int tid = threadIdx.x, lane = tid & 63;
  const int wid = __builtin_amdgcn_readfirstlane(tid >> 6);
  unsigned tch;
  {
    u64 pc;
    unsigned o = ((blockIdx.x >> 3) * 4 + wid + 128 * lane) * 128 + 1024;
    asm volatile("s_cmp_lt_u32 %3, 4\n\ts_cbranch_scc0 .Lpf_skip\n\ts_getpc_b64 %1\n.Lpf_here:\n\tv_min_u32 %2, .Lpf_end - .Lpf_here - 4, %2\n\t"
                 "global_load_dword %0, %2, %1\n.Lpf_skip:"
                 : "=&v"(tch), "=&s"(pc), "+v"(o) : "s"(wid) : "scc", "memory");
  }
  const int b = blockIdx.x, xcd = b & 7, idx = b >> 3;
  const int m1 = (~xcd & 4) * 2 + (idx & 7), n1 = ((xcd & 3) ^ 2) * 4 + (idx >> 3);
  const int blk0 = 22 * (n1 >> 2) + 6 * (n1 & 3) - (n1 == 15 ? 2 : 0);
  const bool big = ((n1 & 3) != 3) && (n1 != 14);
  const int ks = xcd & 3, tm = (xcd >> 2) * 8 + (idx >> 2), tn = idx & 3;
  const int wgl = xcd * 32 + idx;
  constexpr int OOB = 0x40000000;
  asm volatile("" : "+s"(X), "+s"(Cr), "+s"(Ci), "+s"(Hr), "+s"(Hi), "+s"(Wg), "+s"(Wu), "+s"(Wd), "+s"(GW), "+s"(out), "+s"(ws));

  if (__builtin_expect(wid >= 4, 1)) {
    const int lw = wid - 4;
    auto rX = __builtin_amdgcn_make_buffer_rsrc((void*)X, 0, 2048 * 512 * 2, 0x00020000);
    auto rH = __builtin_amdgcn_make_buffer_rsrc((void*)(n1 < 8 ? Hr : Hi), 0, 512 * 512 * 2, 0x00020000);
    auto rG = __builtin_amdgcn_make_buffer_rsrc((void*)Wg, 0, 512 * 1376 * 2, 0x00020000);
    auto rU = __builtin_amdgcn_make_buffer_rsrc((void*)Wu, 0, 512 * 1376 * 2, 0x00020000);
    const int l3 = lane >> 3, l7 = lane & 7;
    const int k1 = m1 + n1;
    auto kblk = [&](int j) { return (j & ~3) | ((j + k1) & 3); };
    int vX[4], vH[2], vW[3];
#pragma unroll
    for (int i = 0; i < 4; ++i) {
      const int v = 32 * lw + 8 * i + l3;
      const int s = 32 * lw + 16 * (i & 1) + 8 * (lane >> 5) + 4 * (i >> 1) + (l3 & 3);
      vX[i] = (m1 * 128 + s) * 1024 + ((l7 ^ ((v >> 1) & 7)) * 16);
    }
    auto issue_x = [&](int j) {
#pragma unroll
      for (int i = 0; i < 4; ++i)
        __builtin_amdgcn_raw_ptr_buffer_load_lds(rX, smem + j % 3 * SLOT + 20480 + (lw * 4 + i) * 1024, 16, vX[i], kblk(j) * 128, 0, 0);
    };
    issue_x(0);
#pragma unroll
    for (int i = 0; i < 2; ++i) {
      const int r = 16 * lw + 8 * i + l3;
      vH[i] = ((n1 & 7) * 64 + r) * 1024 + ((l7 ^ ((r >> 1) & 7)) * 16);
    }
#pragma unroll
    for (int i = 0; i < 3; ++i) {
      const int e = i * 64 + lane, rr = (e * 171) >> 11, s = e - 12 * rr, c = (((s >> 1) ^ (rr >> 3)) << 1) | (s & 1);
      vW[i] = (!big && c >= 8) ? OOB : (16 * lw + rr) * 2752 + blk0 * 32 + c * 16;
    }
    auto issue_w = [&](int j) {
      LAS char* R = smem + j % 3 * SLOT;
#pragma unroll
      for (int i = 0; i < 2; ++i) __builtin_amdgcn_raw_ptr_buffer_load_lds(rH, R + (lw * 2 + i) * 1024, 16, vH[i], kblk(j) * 128, 0, 0);
#pragma unroll
      for (int i = 0; i < 3; ++i) __builtin_amdgcn_raw_ptr_buffer_load_lds(rG, smem + wslot(j) + (lw * 3 + i) * 1024, 16, vW[i], kblk(j) * (64 * 2752), 0, 0);
    };
    auto issue_u = [&](int j) {
      LAS char* R = smem + (j == 0 ? 147456 : j < 4 ? 61440 + j * 12288 : (j - 4) * 12288);
#pragma unroll
      for (int i = 0; i < 3; ++i) __builtin_amdgcn_raw_ptr_buffer_load_lds(rU, R + (lw * 3 + i) * 1024, 16, vW[i], kblk(j) * (64 * 2752), 0, 0);
    };
    if (!big && lw == 0) {
      auto rGW = __builtin_amdgcn_make_buffer_rsrc((void*)GW, 0, 1024, 0x00020000);
      __builtin_amdgcn_raw_ptr_buffer_load_lds(rGW, smem + 159744, 16, kblk(l3) * 128 + l7 * 16, 0, 0, 0);
    }
    issue_w(0); issue_x(1);
    auto rC = __builtin_amdgcn_make_buffer_rsrc((void*)(ks < 2 ? Cr : Ci), 0, 2048 * 2048 * 2, 0x00020000);
    auto rW = __builtin_amdgcn_make_buffer_rsrc((void*)ws, 0, WS_END, 0x00020000);
    auto rD = __builtin_amdgcn_make_buffer_rsrc((void*)Wd, 0, 1376 * 512 * 2, 0x00020000);
    int vC[4], vD[4];
#pragma unroll
    for (int i = 0; i < 4; ++i) {
      const int v = 32 * lw + 8 * i + l3;
      const int s = 32 * lw + 16 * (i & 1) + 8 * (lane >> 5) + 4 * (i >> 1) + (l3 & 3);
      vC[i] = (tm * 128 + (s ^ ((ks & 2) * 32))) * 4096 + (ks & 1) * 2048 + ((l7 ^ ((v >> 1) & 7)) * 16);
      const int r = 8 * i + l3;
      vD[i] = r * 1024 + (tn * 128 + (ks & 1) * 64) * 2 + (((l7 >> 1) ^ ((r >> 1) & 3)) * 32) + (l7 & 1) * 16;
    }
    int vL = lane * 16;
    const int sB = WS_AB + (((ks >> 1) * 2 + (tn >> 1)) * 64 + (ks & 1) * 32) * 16384 + ((lw >> 1) * 16 + (tn & 1) * 8 + ((lw ^ ks) & 1) * 4) * 1024;
    const int sHb = WS_HB + (tm * 2 + (ks >> 1)) * (43 * 4096);
    auto linear = [&](auto Nc, LAS char* R, int v, int so) LAM {
      sfor<0, decltype(Nc)::value>([&](auto Ic) LAM { __builtin_amdgcn_raw_ptr_buffer_load_lds(rW, R, 16, v, so, decltype(Ic)::value * 1024, 16); });
    };
    const int k2 = tn + 2 * ((tm >> 2) & 1), k3 = (7 * tn + ((ND * (tm & 7)) >> 3)) % ND + lw;
    auto kk = [&](int st) { return (((st >> 2) + tm) & 3) * 4 + ((st + k2) & 3); };
    auto issue_A = [&](int st) {
      LAS char* R = smem + ((st & 3) == 1 ? 9 : (st & 3) == 2 ? 6 : (st & 3) == 3 ? 1 : st == 4 ? 8 : st == 8 ? 0 : 7) * 16384 + lw * 4096;
#pragma unroll
      for (int i = 0; i < 4; ++i) __builtin_amdgcn_raw_ptr_buffer_load_lds(rC, R + i * 1024, 16, vC[i], kk(st) * 128, 0, 0);
    };
    auto issue_B = [&](int st) { linear(IC<4>{}, smem + 32768 + ((st + 3) & 3) * 16384 + lw * 4096, vL, sB + kk(st) * 32768); };
    auto issue_T = [&](int d, int part) {
      const int sl = d % 5; int kq = k3 + 4 * d; kq -= kq >= ND ? ND : 0;
      if (part & 1) linear(IC<4>{}, smem + (sl == 0 ? 7 : sl == 1 ? 6 : sl == 2 ? 1 : sl == 3 ? 8 : 9) * 16384 + lw * 4096, vL, sHb + kq * 4096);
      LAS char* R = smem + (sl ? 32768 + (sl & 3) * 16384 : 0) + lw * 4096;
#pragma unroll
      for (int i = 0; i < 4; ++i) if (part & 2) __builtin_amdgcn_raw_ptr_buffer_load_lds(rD, R + i * 1024, 16, vD[i], kq * 32768, 0, 0);
    };
    auto issue_E = [&](int e, int part) {
      const int kq = ND + 4 * ks + ((2 * e + (lw >> 1) + tn) & 3);
      if (part & 1) {
        const int so = WS_HB + ((tm * 2 + (((ks >> 1) ^ lw) & 1)) * 43 + kq) * 4096;
        sfor<0, 4>([&](auto Ic) LAM {
          constexpr int i = decltype(Ic)::value;
          __builtin_amdgcn_raw_ptr_buffer_load_lds(rW, smem + (8 + e) * 16384 + (lw & 1) * 8192 + (lw >> 1) * 1024 + i * 1024, 16, vL, so, i * 1024, 16);
        });
      }
      const int so = kq * 32768 + (lw & 1) * (128 - (ks & 1) * 256);
#pragma unroll
      for (int i = 0; i < 4; ++i) if (part & 2) __builtin_amdgcn_raw_ptr_buffer_load_lds(rD, smem + (e ? 2 : 5) * 16384 + lw * 4096 + i * 1024, 16, vD[i], so, 0, 0);
    };
    const int selh = tm & 3, sel[4] = {selh, selh == 2 ? 3 : selh, selh, selh};
    u32x4 hv[6];
    u64 tag;
    constexpr int P[4] = {5, 9, 14, 18};
    auto leave = [&](auto Kc, auto Lc, auto Nc) LAM {
      constexpr int K = decltype(Kc)::value, L = decltype(Lc)::value, N = decltype(Nc)::value;
      int n = 0;
      if constexpr (K >= PH && L <= PH + 3) n += PH + selh >= L && PH + selh <= K;
      sfor<0, 4>([&](auto Gc) LAM {
        constexpr int G = decltype(Gc)::value;
        if constexpr (K >= P[G] && L <= P[G] + 4) n += P[G] + sel[G] >= L && P[G] + sel[G] <= K;
      });
      if (n == 0) wait_vm<N>(); else if (n == 1) wait_vm<N + 4>(); else if (n == 2) wait_vm<N + 8>(); else wait_vm<N + 12>();
    };
    auto fin = [&](auto Kc, auto Lc, auto Nc) LAM {
      constexpr int K = decltype(Kc)::value, L = decltype(Lc)::value, N = decltype(Nc)::value;
      if constexpr (K >= PH && K <= PH + 3) {
        if (K == PH + selh) {
          const int sHp = WS_HB + ((m1 * 2 + (lw >> 1)) * 43 + (blk0 >> 1)) * 4096 + (lw & 1) * 2048;
#pragma unroll
          for (int t = 0; t < 6; ++t)
            if (t < 4 || big) __builtin_amdgcn_raw_buffer_store_b128(hv[t], rW, vL + (t & 1) * 1024, sHp + (t >> 1) * 4096, 16);
          sent();
        }
      }
      sfor<0, 4>([&](auto Gc) LAM {
        constexpr int G = decltype(Gc)::value;
        if constexpr (K >= P[G] && K <= P[G] + 4) {
          if (K == P[G] + sel[G]) {
            constexpr int R = (ND + 10) / 3, r0 = (G - 1) * R;
            if constexpr (G >= 1) {
              const int t = r0 + lane;
              const int a = t < 8 ? (ND * 32 + ks * 128 + idx * 4 + (t >> 1)) * 1024 + (t & 1) * 128 : (idx * ND + t - 8) * 1024 + (ks & 1) * 128;
              asm volatile("buffer_load_dword %0, %1, %2, 0 offen" : "+v"(tch) : "v"(lane < R && t < ND + 8 ? a + lw * 256 : OOB), "s"(rD) : "memory");
            }
            issue_A(4 * G);
          }
        }
      });
      if constexpr (N >= 0) leave(Kc, Lc, Nc);
      if constexpr (L - 1 >= PH && L - 1 <= PH + 3 && K > 16) {
        if (L - 1 == PH + selh && lane == 0) __hip_atomic_store((gu64*)(ws + WS_FH) + ((m1 * 16 + n1) * 4 + lw), tag, RLX_AGENT);
      }
    };
    wait_vm<4>(); BARRIER();
    issue_w(1);
    const char* wse = ws;
    asm volatile("" : "+s"(wse));
    u64 epoch = *(const __attribute__((address_space(4))) u64*)(wse + WS_E);
    sfor<0, 6>([&](auto Kc) LAM {
      constexpr int k = decltype(Kc)::value;
      issue_x(k + 2); issue_w(k + 2);
      if (k == 0) asm volatile("" : "+s"(epoch));
      fin(Kc, IC<k - 1>{}, IC<9>{}); BARRIER();
    });
    issue_u(0);
    issue_u(1); fin(IC<6>{}, IC<5>{}, IC<6>{}); BARRIER();
    issue_u(2); issue_u(3); issue_u(4); fin(IC<7>{}, IC<6>{}, IC<12>{}); BARRIER();
    tag = epoch * 6364136223846793005ull + 1442695040888963407ull;
    const gu64* fa = (const gu64*)(ws + WS_FA) + ((((ks & 1) * 8 + l3) * 16 + (ks >> 1) * 8 + tn * 2 + ((lane >> 2) & 1)) * 4 + (lane & 3));
    u64 pv;
    fin(IC<8>{}, IC<6>{}, IC<9>{}); BARRIER();
    issue_u(5); issue_u(6); fin(IC<9>{}, IC<7>{}, IC<12>{}); BARRIER();
    issue_u(7); fin(IC<10>{}, IC<7>{}, IC<9>{}); BARRIER();
    issue_A(1); fin(IC<11>{}, IC<10>{}, IC<4>{}); BARRIER();
    asm volatile("global_load_dwordx2 %0, %1, off sc1" : "=v"(pv) : "v"(fa) : "memory");
    const bool first = (l3 >> 1) == (tm & 3);
    issue_A(2);
    asm volatile("s_waitcnt vmcnt(4)" : "+v"(pv) :: "memory");
    bool early = __all(!first || pv == tag);
    if (!early) {
      asm volatile("global_load_dwordx2 %0, %1, off sc1\n\ts_waitcnt vmcnt(0)" : "=v"(pv) : "v"(fa) : "memory");
      early = __all(!first || pv == tag);
    }
    if (early) issue_B(0);
    fin(IC<12>{}, IC<12>{}, IC<-1>{}); BARRIER();
    issue_A(3);
    if (!early) { spin_flags(fa, first, tag); issue_B(0); }
    issue_B(1); fin(IC<13>{}, IC<13>{}, IC<-1>{});
    if (early) leave(IC<13>{}, IC<13>{}, IC<8>{}); else leave(IC<13>{}, IC<13>{}, IC<4>{});
    BARRIER();
#pragma unroll
    for (int t = 0; t < 6; ++t) hv[t] = *(LAS const u32x4*)(smem + (t < 4 ? 65536 + t * 1024 : 45056 + t * 1024) + lw * 4096 + lane * 16);
    asm volatile("global_load_dwordx2 %0, %1, off sc1" : "=v"(pv) : "v"(fa) : "memory");
    asm volatile("s_waitcnt lgkmcnt(0)" : "+v"(hv[0]), "+v"(hv[1]), "+v"(hv[2]), "+v"(hv[3]), "+v"(hv[4]), "+v"(hv[5]) :: "memory");
    issue_B(2); fin(IC<14>{}, IC<13>{}, IC<5>{}); BARRIER();
    issue_B(3); leave(IC<14>{}, IC<14>{}, IC<8>{});
    asm volatile("" : "+v"(pv) :: "memory");
    if (!__all(pv == tag)) spin_flags(fa, true, tag);
    issue_B(4); fin(IC<15>{}, IC<14>{}, IC<8>{}); BARRIER();
    issue_A(5); issue_B(5); fin(IC<16>{}, IC<15>{}, IC<12>{}); BARRIER();
    const gu64* fh = (const gu64*)(ws + WS_FH) + (tm * 64 + lane);
    sfor<2, 12>([&](auto Jc) LAM {
      constexpr int j = decltype(Jc)::value;
      if constexpr (j == 9) asm volatile("global_load_dwordx2 %0, %1, off sc1" : "=v"(pv) : "v"(fh) : "memory");
      if constexpr (j & 3) issue_A(j + 4);
      issue_B(j + 4);
      if constexpr (j == 8) issue_T(0, 2);
      fin(IC<15 + j>{}, IC<13 + j>{}, IC<((j & 3) == 0 || (j & 3) == 1 ? 12 : 16) + (j == 9 || j == 10) + (j == 8 || j == 9 ? 4 : 0)>{});
      if constexpr (j == 11) asm volatile("" : "+v"(pv) :: "memory");
      BARRIER();
    });
    asm volatile("" :: "v"(tch));
    issue_E(0, 2);
    if (!__all(pv == tag)) spin_flags(fh, true, tag);
    sfor<1, 12>([&](auto Ic) LAM {
      constexpr int i = decltype(Ic)::value, d = i - 5;
      constexpr int DCUT[12] = {1, DSCHED}, lo = DCUT[i - 1], hi = DCUT[i];
      sfor<lo, hi>([&](auto Hc) LAM {
        constexpr int h = decltype(Hc)::value;
        if (h == 15 && lw == 3) {
          vL = OOB;
#pragma unroll
          for (int q = 0; q < 4; ++q) vD[q] = OOB;
        }
        if constexpr (h < 2) issue_E(0, 1); else if constexpr (h == 2) issue_T(0, 1); else if constexpr (h < 5) issue_E(1, h - 2);
        else issue_T((h - 3) / 2, 1 + (h & 1));
      });
      constexpr int need = i <= 3 ? i - 1 : d < 0 ? 5 : 5 + 2 * d;
      static_assert(hi <= (d <= 1 ? 2 * i + 1 : 2 * d + 9) && hi >= need, "slot free / step issued");
      wait_vm<4 * (hi - need) + (i == 1 ? 8 : 0)>();
      BARRIER();
    });
    if (b == 0 && wid == 7) {
#pragma unroll 1
      for (int q = 0; q < 4; ++q) spin_flags((const gu64*)(ws + WS_FA) + (q * 64 + lane) * 4, true, tag);
      if (lane == 0) __hip_atomic_store((gu64*)(ws + WS_E), tag, RLX_AGENT);
    }
  } else {
    const int w = wid;
    const char* wse = ws;
    asm volatile("" : "+s"(wse));
    const u64 epoch = *(const __attribute__((address_space(4))) u64*)(wse + WS_E);
    const u64 tag = epoch * 6364136223846793005ull + 1442695040888963407ull;
    auto rW = __builtin_amdgcn_make_buffer_rsrc((void*)ws, 0, WS_END, 0x00020000);
    const unsigned lds0 = (unsigned)(size_t)smem;
    auto rd128 = [&](unsigned a, int o) { return *(LAS const bf16x8*)((LAS char*)(size_t)a + o); };
    auto rdtr = [&](unsigned a, int o, int d) {
      bf16x4 lo = __builtin_amdgcn_ds_read_tr16_b64_v4bf16((LAS bf16x4*)((LAS char*)(size_t)a + o));
      bf16x4 hi = __builtin_amdgcn_ds_read_tr16_b64_v4bf16((LAS bf16x4*)((LAS char*)(size_t)a + o + d));
      return __builtin_shufflevector(lo, hi, 0, 1, 2, 3, 4, 5, 6, 7);
    };
    const int wm = w >> 1, wn = w & 1;
    unsigned aX[2], aD0[2];
    const unsigned vl = opq(lane * 16);
    aD0[0] = opq(lds0 + (lane >> 2) * 128 + (((2 * wn) ^ ((lane >> 3) & 3)) * 32) + (lane & 3) * 8 + 32768);
    int sY = WS_YP + wgl * 24576 + w * 2048;
    asm volatile("" : "+s"(sY));
    auto stage1 = [&](auto NBc) LAM {
      constexpr int NB = decltype(NBc)::value, NF = NB == 4 ? 9 : 10, D = 4;
      const int i16 = lane & 15, g = lane >> 4;
      unsigned aH[2], aW[2];
#pragma unroll
      for (int s = 0; s < 2; ++s) {
        aH[s] = opq(lds0 + i16 * 128 + (((4 * s + g) ^ ((i16 >> 1) & 7)) * 16)); aX[s] = opq(aH[s] + w * 4096);
        aW[s] = opq(lds0 + (8 * g + (i16 >> 2)) * 192 + ((s ^ (g & 1)) * 32) + (lane & 3) * 8 + wslot(0));
      }
      const unsigned aG = opq(lds0 + g * 16 + 159744);
      bf16x8 xf[16][2], fr[8];
      f32x4 G[NB][2];
      asm volatile("" :: "s"(tag));
      WAIT_LGKM0();
      BARRIER();
      __builtin_amdgcn_s_setprio(3);
      {
        f32x4 ab[2][4], z[2];
        auto rdF = [&](auto Fc) LAM {
          constexpr int F = decltype(Fc)::value, kq = F / NF, j = kq >> 1, s = kq & 1, f = F % NF;
          if (j && !s && !f) {
            constexpr int dl = j % 3 ? SLOT : -2 * SLOT;
#pragma unroll
            for (int t = 0; t < 2; ++t) { aX[t] = opq(aX[t] + dl); aH[t] = opq(aH[t] + dl); aW[t] = opq(aW[t] + (wslot(j) - wslot(j - 1))); }
          }
          if (f == 0) { xf[kq][0] = rd128(aX[s], 20480); xf[kq][1] = rd128(aX[s], 22528); }
          if constexpr (f < 4) fr[F & 7] = rd128(aH[s], f * 2048);
          else if constexpr (f < 4 + NB) fr[F & 7] = rdtr(aW[f & 1], ((f - 4) >> 1) * 64 + s * 6144, 768);
          else fr[F & 7] = rd128(aG, kq * 64);
        };
        auto mm = [&](auto Fc, int mb) LAM {
          constexpr int F = decltype(Fc)::value, kq = F / NF, f = F % NF;
          if constexpr (f < 4) mfx<kq == 0>(ab[mb][f], xf[kq][mb], fr[F & 7]);
          else if constexpr (f < 4 + NB) mfx<kq == 0>(G[f - 4][mb], fr[F & 7], xf[kq][mb]);
          else mfx<kq == 0>(z[mb], fr[F & 7], xf[kq][mb]);
        };
        sfor<0, D>(rdF);
        SB();
        sfor<0, 16 * NF>([&](auto Fc) LAM {
          constexpr int F = decltype(Fc)::value;
          if constexpr (F + D < 16 * NF && ((F + D) % (2 * NF)) == 0) { BARRIER(); SB(); }
          mm(Fc, 0); SB();
          if constexpr (F + D < 16 * NF) rdF(IC<F + D>{});
          SB();
          mm(Fc, 1); SB();
        });
        WAIT_LGKM0(); BARRIER();
        settle();
#pragma unroll
        for (int c = 0; c < 4; ++c) { pin(ab[0][c]); pin(ab[1][c]); }
        if constexpr (NB == 4) { pin(z[0]); pin(z[1]); }
        asm volatile("s_waitcnt vmcnt(0)" :: "v"(tch) : "memory");
        const unsigned sgn = n1 < 8 ? 0u : 0x80008000u;
        const int sA = WS_AB + (((n1 >> 2) * 64 + m1 * 4 + w) * 16 + (n1 & 3) * 4) * 1024;
#pragma unroll
        for (int c = 0; c < 4; ++c) {
          u32x4 v = {cvtpk(ab[0][c][0], ab[0][c][1]) ^ sgn, cvtpk(ab[0][c][2], ab[0][c][3]) ^ sgn,
                     cvtpk(ab[1][c][0], ab[1][c][1]) ^ sgn, cvtpk(ab[1][c][2], ab[1][c][3]) ^ sgn};
          SB();
          __builtin_amdgcn_raw_buffer_store_b128(v, rW, vl + c * 1024, sA, 16);
          sent();
        }
        if (NB == 4 && n1 == 3 && lane < 16) {
#pragma unroll
          for (int mb = 0; mb < 2; ++mb) {
            float gv = __builtin_amdgcn_rcpf(1.0f + __builtin_amdgcn_exp2f(-1.4426950409f * z[mb][0]));
            __hip_atomic_store((gu32*)(ws + WS_G) + m1 * 128 + w * 32 + lane * 2 + mb, __builtin_bit_cast(unsigned, gv), RLX_AGENT);
          }
        }
        SB();
      }
      {
        gu64* const fa = (gu64*)(ws + WS_FA) + ((m1 * 16 + n1) * 4 + w);
        f32x4 U[NB][2];
        float t, r;
        auto rdF = [&](auto Fc) LAM {
          constexpr int F = decltype(Fc)::value, kq = F / NB, j = kq >> 1, s = kq & 1, f = F % NB;
          if ((j == 0 || j == 1 || j == 4) && !s && !f) {
            constexpr int dl = j == 0 ? 147456 - wslot(7) : -73728;
#pragma unroll
            for (int c = 0; c < 2; ++c) aW[c] = opq(aW[c] + dl);
          }
          if (kq >= 14 && f == 0) { xf[kq][0] = rd128(aX[s], 20480); xf[kq][1] = rd128(aX[s], 22528); }
          fr[F & 7] = rdtr(aW[f & 1], (j == 0 ? 0 : j < 4 ? j - 1 : j - 4) * 12288 + (f >> 1) * 64 + s * 6144, 768);
        };
        auto act = [&](auto Fc) LAM {
          constexpr int F = decltype(Fc)::value, v = F >> 2, ph = F & 3;
          if constexpr (ph == 0 && v < 4 * NB) asm volatile("v_fmaak_f32 %0, %1, %1, 0x41b2e92f\n\tv_mul_f32 %0, %1, %0" : "=&v"(t) : "v"(G[v >> 2][0][v & 3]));
          if constexpr (ph == 1) {
            if constexpr (v < 4 * NB) asm volatile("v_mul_f32 %0, 0xbdd2d3e8, %0" : "+v"(t));
            if constexpr (v > 0) asm volatile("v_mul_f32 %0, %0, %1" : "+v"(G[(v - 1) >> 2][0][(v - 1) & 3]) : "v"(r));
          }
          if constexpr (ph == 2) asm volatile("v_exp_f32 %0, %0" : "+v"(t));
          if constexpr (ph == 3) asm volatile("v_add_f32 %1, 1.0, %1\n\tv_rcp_f32 %0, %1" : "=v"(r), "+v"(t));
        };
        sfor<0, D>(rdF);
        SB();
        sfor<0, 16 * NB>([&](auto Fc) LAM {
          constexpr int F = decltype(Fc)::value, f = F % NB;
          if (F + D == FA_KQ * NB) { wait_vm<0>(); if (lane == 0) __hip_atomic_store(fa, tag, RLX_AGENT); SB(); }
          if constexpr (((F + D) % (2 * NB)) == 0 && ((0x2e >> ((F + D) / (2 * NB))) & 1)) { BARRIER(); SB(); }
          mfx<F / NB == 0>(U[f][0], fr[F & 7], xf[F / NB][0]); SB();
          if constexpr (F + D < 16 * NB) rdF(IC<F + D>{});
          SB();
          mfx<F / NB == 0>(U[f][1], fr[F & 7], xf[F / NB][1]); SB();
          act(Fc);
          SB();
        });
        WAIT_LGKM0(); BARRIER();
        settle();
        act(IC<16 * NB + 1>{});
#pragma unroll
        for (int c = 0; c < NB; ++c) { pin(U[c][0]); pin(U[c][1]); }
        LAS char* const hs = (LAS char*)(size_t)opq(lds0 + w * 4096 + 49152 + vl);
        f32x2 c1 = {-2.302208198f, -2.302208198f}, c2 = {-0.1029432397f, -0.1029432397f};
        asm volatile("" : "+v"(c1), "+v"(c2));
        u32x4 hp[NB / 2][2];
        auto half0 = [&](int i) {
          const f32x4 h0 = G[2 * i][0] * U[2 * i][0], h1 = G[2 * i + 1][0] * U[2 * i + 1][0];
          hp[i][0] = u32x4{cvtpk(h0[0], h0[1]), cvtpk(h0[2], h0[3]), cvtpk(h1[0], h1[1]), cvtpk(h1[2], h1[3])};
          SB();
        };
        auto half1 = [&](int i) {
          const f32x4 g0 = G[2 * i][1], u0 = U[2 * i][1], g1 = G[2 * i + 1][1], u1 = U[2 * i + 1][1];
          hp[i][1] = u32x4{geglu(g0.xy, u0.xy, c1, c2), geglu(g0.zw, u0.zw, c1, c2), geglu(g1.xy, u1.xy, c1, c2), geglu(g1.zw, u1.zw, c1, c2)};
          SB();
        };
        auto put = [&](int i, int mb) { *(LAS u32x4*)(hs + (i < 2 ? 16384 + i * 2048 : 0) + mb * 1024) = hp[i][mb]; SB(); };
#pragma unroll
        for (int i = 0; i < NB / 2; ++i) { half0(i); put(i, 0); half1(i); put(i, 1); }
        WAIT_LGKM0();
        SB();
      }
    };
    if (big) stage1(IC<6>{}); else stage1(IC<4>{});
    unsigned aH0, aH1, aD1[2];
    bf16x8 hf[4][2], df[4][2];
    auto rh = [&](int sl, int j, int mb) { hf[j][mb] = rd128(sl == 2 ? aH0 : aH1, (sl == 1 ? 0 : sl < 3 ? 1 : sl - 1) * 16384 + j * 4096 + mb * 1024); };
    auto rd = [&](int sl, int j, int c) { df[j][c] = rdtr(sl == 0 ? aD1[c] : aD0[c], (sl & 3) * 16384 + j * 4096, 2048); };
    f32x4 own[2][2];
    u32x4 pick[2];
    int vO, sZ[3];
    gu64* fy; const gu64* fz;
    float sc[64];
    f32x4 ac2[4][4];
    float gn[4];
    f32x2 gv[2];
    {
      unsigned aA[2], aL[2], aZ[2];
#pragma unroll
      for (int s = 0; s < 2; ++s) aZ[s] = opq(aX[s] + (114688 - SLOT + (wm - w) * 4096));
      const unsigned aB = opq(vl + (lds0 + wn * 2048 + 32768));
      f32x4 acc[4][4];
      bf16x8 fa[2][4], fb[2][4];
      auto rdA = [&](int set, int mb, int slot, int s) { fa[set][mb] = rd128(slot == 0 ? aZ[s] : slot == 3 ? aL[s] : aA[s], (slot == 1 ? 49152 : slot == 3 ? 16384 : 0) + (mb >> 1) * 8192 + (mb & 1) * 2048); };
      auto rdB = [&](int set, int nb, int slot, int s) { fb[set][nb] = rd128(aB, ((slot + 3) & 3) * 16384 + s * 8192 + (nb >> 1) * 4096 + (nb & 1) * 1024); };
      auto half = [&](int u, int s) LAM {
        const int cur = s, nxt = s ^ 1, ns = s ^ 1, nslot = s ? ((u + 1) & 3) : u;
        auto mm = [&](int nb, int mb) LAM { mfa(acc[nb][mb], fb[cur][nb], fa[cur][mb]); SB(); };
        if (s == 1) { WAIT_LGKM0(); BARRIER(); SB(); }
        mm(0, 0); rdA(nxt, 0, nslot, ns); SB();
        mm(1, 0); rdB(nxt, 0, nslot, ns); SB();
        mm(2, 0); rdB(nxt, 1, nslot, ns); SB();
        mm(3, 0); rdB(nxt, 2, nslot, ns); SB();
        mm(0, 1); rdB(nxt, 3, nslot, ns); SB();
        mm(1, 1); rdA(nxt, 1, nslot, ns); SB();
        mm(2, 1); rdA(nxt, 2, nslot, ns); SB();
        mm(3, 1); rdA(nxt, 3, nslot, ns); SB();
        mm(0, 2); mm(1, 2); mm(2, 2); mm(3, 2);
        mm(0, 3); mm(1, 3); mm(2, 3); mm(3, 3);
      };
      BARRIER();
      rdA(0, 0, 0, 0);
#pragma unroll
      for (int i = 0; i < 4; ++i) rdB(0, i, 0, 0);
#pragma unroll
      for (int i = 1; i < 4; ++i) rdA(0, i, 0, 0);
      SB();
#pragma unroll
      for (int s = 0; s < 2; ++s) { aL[s] = opq(aZ[s] - 114688); aA[s] = opq(aZ[s] - 16384); }
      aH0 = opq(vl + (lds0 + wm * 2048)); aH1 = opq(aH0 + 98304);
      aD0[1] = opq(aD0[0] ^ 32); aD1[0] = opq(aD0[0] - 32768); aD1[1] = opq(aD0[1] - 32768);
#pragma unroll
      for (int i = 0; i < 16; ++i) { acc[i >> 2][i & 3] = f32x4{}; pin(acc[i >> 2][i & 3]); }
      bf16x4 fw[8];
      asm volatile("" : "=v"(gv[0]), "=v"(gv[1]), "=v"(fw[0]), "=v"(fw[1]), "=v"(fw[2]), "=v"(fw[3]), "=v"(fw[4]), "=v"(fw[5]), "=v"(fw[6]), "=v"(fw[7]));
      const unsigned aE = opq(aH0 + wm * 2048 + 131072), vG = opq((vl & 240) >> 1);
      {
        const int lane = vl >> 4, g = lane >> 4, q = (lane & 15) >> 2, p = lane & 3;
        const unsigned one = g == q ? 0x3f80u << (16 * (p & 1)) : 0u, o0 = p < 2 ? one : 0u, o1 = one - o0;
        pick[0] = u32x4{o0, o1, 0u, 0u}; pick[1] = u32x4{0u, 0u, o0, o1};
        vO = ((tm * 128 + (ks >> 1) * 64 + wm * 32 + 8 * q + p) * 512 + tn * 128 + (ks & 1) * 64 + wn * 32 + (g & 1) * 16 + (g >> 1) * 8) * 2;
        fy = (gu64*)(ws + WS_FY) + (wgl * 4 + w) * 8;
        fz = (const gu64*)(ws + WS_FY) + ((((xcd & 4) | p) * 32 + idx) * 4 + w) * 8;
#pragma unroll
        for (int o = 0; o < 3; ++o) sZ[o] = WS_YP + (((xcd ^ (o + 1)) * 32 + idx) * 3 + o) * 8192 + w * 2048;
        asm volatile("" : "+v"(pick[0]), "+v"(pick[1]), "+v"(vO), "+s"(fy), "+v"(fz), "+s"(sZ[0]), "+s"(sZ[1]), "+s"(sZ[2]));
      }
      SB();
#pragma unroll 1
      for (int it = 0; it < 4; ++it) {
        half(0, 0); half(0, 1); half(1, 0); half(1, 1);
        { const unsigned z = it == 0 ? 131072 : it == 1 ? 0 : 114688; aZ[0] = opq(aL[0] + z); aZ[1] = opq(aL[1] + z); SB(); }
        if (it == 3) {
          const int sG = WS_G + (tm * 128 + (ks >> 1) * 64 + wm * 32) * 4;
          asm volatile("buffer_load_dwordx2 %0, %3, %4, %5 offen sc1\n\tbuffer_load_dwordx2 %1, %3, %4, %6 offen sc1\n\tv_mov_b32 %2, %7"
                       : "+v"(gv[0]), "+v"(gv[1]), "+v"(aZ[0]) : "v"(vG), "s"(rW), "s"(sG), "s"(sG ^ 256), "v"(aE) : "memory");
          SB();
        }
        half(2, 0); half(2, 1);
        if (it == 3) {
#pragma unroll
          for (int i = 0; i < 8; ++i)
            asm volatile("ds_read_b64_tr_b16 %0, %1 offset:%2" : "+v"(fw[i]) : "v"(aD0[(i >> 1) & 1]), "n"(49152 + (i >> 2) * 4096 + (i & 1) * 2048) : "memory");
          SB();
        }
        half(3, 0); half(3, 1);
      }
      auto rdE = [&](int set, int mb, int e, int s) { fa[set][mb] = rd128(aE, e * 16384 + s * 1024 + (mb >> 1) * 8192 + (mb & 1) * 2048); };
      auto rdW = [&](int set, int nb, int e, int s) { fb[set][nb] = rdtr(aD0[nb & 1], (e ? 0 : 49152) + s * 8192 + (nb >> 1) * 4096, 2048); };
#pragma unroll
      for (int i = 0; i < 4; ++i) fb[0][i] = __builtin_shufflevector(fw[2 * i], fw[2 * i + 1], 0, 1, 2, 3, 4, 5, 6, 7);
      SB();
      sfor<0, 3>([&](auto Hc) LAM {
        constexpr int H = decltype(Hc)::value, cur = H & 1, nxt = cur ^ 1, e = (H + 1) >> 1;
        auto mm = [&](int nb, int mb) LAM { mfx<H == 0>(ac2[nb][mb], fb[cur][nb], fa[cur][mb]); SB(); };
        auto ra = [&](int i) LAM { rdE(nxt, i, e, nxt); SB(); };
        auto rb = [&](int i) LAM { rdW(nxt, i, e, nxt); SB(); };
        auto gs = [&](int i, int c) LAM {
#pragma unroll
          for (int n = H * 20 - 8 + i; n < H * 20 - 8 + i + c; ++n) {
            const int k = n + (n < 8 ? 8 : 16);
            if (n >= 0 && n < 48) asm volatile("v_mul_f32 %0, %1, %0" : "+v"(sc[k]) : "v"(gv[(k >> 3) & 1][(k >> 2) & 1]));
            if (n >= 48) asm volatile("v_sub_f32 %0, 1.0, %1" : "=v"(gn[n - 48]) : "v"(gv[(n >> 1) & 1][n & 1]));
          }
          SB();
        };
        if (cur == 1) { WAIT_LGKM0(); BARRIER(); SB(); }
        mm(0, 0); ra(0); gs(0, 1);
        mm(1, 0); rb(0);
        mm(2, 0); rb(1);
        mm(3, 0); rb(2);
        mm(0, 1); rb(3);
        mm(1, 1); ra(1); gs(1, 1);
        mm(2, 1); ra(2); gs(2, 1);
        mm(3, 1); ra(3); gs(3, 1);
        mm(0, 2); gs(4, 2);
        mm(1, 2); gs(6, 2);
        if (H == 0) {
          asm volatile("s_waitcnt vmcnt(0)" : "+v"(gv[0]), "+v"(gv[1]) :: "memory");
#pragma unroll
          for (int i = 0; i < 16; ++i) pin(acc[i >> 2][i & 3]);
#pragma unroll
          for (int i = 0; i < 64; ++i) sc[i] = acc[i >> 4][(i >> 2) & 3][i & 3];
          SB();
        }
        mm(2, 2); gs(8, 2);
        mm(3, 2); gs(10, 2);
        mm(0, 3); gs(12, 2);
        mm(1, 3); gs(14, 2);
        mm(2, 3); gs(16, 2);
        mm(3, 3); gs(18, 2);
      });
      WAIT_LGKM0(); BARRIER(); SB();
      u32x4 yq[6];
      sfor<0, 18>([&](auto Mc) LAM {
        constexpr int M = decltype(Mc)::value, o = ((M >> 2) + 1) & 3, C = M - 6, oc = (C >> 2) + 1;
        if constexpr (M < 16) { mfa(ac2[(o & 1) * 2 + (M & 1)][(o & 2) + ((M >> 1) & 1)], fb[1][(o & 1) * 2 + (M & 1)], fa[1][(o & 2) + ((M >> 1) & 1)]); SB(); }
        if constexpr (M == 0 || M == 2 || M == 3 || M == 4) rd(0, M > 2, M & 1 ^ (M >= 2));
        if constexpr (M == 1 || M == 5) { rh(0, M >> 2, 0); rh(0, M >> 2, 1); }
        if constexpr (C >= 0) {
          constexpr int nb = (oc & 1) * 2 + (C & 1), mb = (oc & 2) + ((C >> 1) & 1), k = nb * 16 + mb * 4;
          asm volatile("v_fmac_f32 %2, %6, %7\n\tv_fmac_f32 %3, %6, %8\n\tv_fmac_f32 %4, %6, %9\n\tv_fmac_f32 %5, %6, %10\n\t"
                       "v_cvt_pk_bf16_f32 %0, %2, %3\n\tv_cvt_pk_bf16_f32 %1, %4, %5"
                       : "=&v"(yq[C >> 1][(C & 1) * 2]), "=v"(yq[C >> 1][(C & 1) * 2 + 1]), "+v"(sc[k]), "+v"(sc[k + 1]), "+v"(sc[k + 2]), "+v"(sc[k + 3])
                       : "v"(gn[mb]), "v"(ac2[nb][mb][0]), "v"(ac2[nb][mb][1]), "v"(ac2[nb][mb][2]), "v"(ac2[nb][mb][3]));
          if constexpr (C & 1) { SB(); __builtin_amdgcn_raw_buffer_store_b128(yq[C >> 1], rW, vl + ((C >> 1) & 1) * 1024, sY + (C >> 2) * 8192, 16); sent(); }
        }
        SB();
      });
    }
    {
      const int lane = vl >> 4;
      f32x4 acc[2][2];
      u32x4 y[3][2];
      const bool fzl = (lane & 3) != ks;
      u64 pv; bool late = true;
      auto rO = __builtin_amdgcn_make_buffer_rsrc((void*)out, 0, 2048 * 512 * 2, 0x00020000);
      asm volatile("" : "+s"(rO));
      auto load_y = [&]() {
#pragma unroll
        for (int o = 0; o < 3; ++o) {
#pragma unroll
          for (int mb = 0; mb < 2; ++mb)
            asm volatile("buffer_load_dwordx4 %0, %1, %2, %3 offen offset:%4 sc1" : "=v"(y[o][mb]) : "v"(vl), "s"(rW), "s"(sZ[o]), "n"(mb * 1024) : "memory");
        }
      };
      auto kstep = [&](auto Fc, int j, int sl, int jn) LAM {
        constexpr bool F = decltype(Fc)::value;
        mfx<F>(acc[0][0], df[j][0], hf[j][0]); SB(); if (jn >= 0) rd(sl, jn, 0); SB();
        mfx<F>(acc[1][0], df[j][1], hf[j][0]); SB(); if (jn >= 0) { rh(sl, jn, 0); rh(sl, jn, 1); } SB();
        mfx<F>(acc[0][1], df[j][0], hf[j][1]); SB(); if (jn >= 0) rd(sl, jn, 1); SB();
        mfx<F>(acc[1][1], df[j][1], hf[j][1]); SB();
      };
      auto check = [&]() LAM {
        asm volatile("s_waitcnt vmcnt(0)" : "+v"(pv) :: "memory");
        late = !__all(!fzl || pv == tag);
        if (!late) load_y();
        SB();
      };
      auto put = [&](auto Pc) LAM {
        constexpr int P = decltype(Pc)::value;
        if constexpr (P < 16) {
          constexpr int nb = (P >> 2) & 1, mb = (P >> 1) & 1, e = (P & 1) * 2, k = nb * 16 + mb * 4 + e;
          if constexpr (P < 8) asm volatile("v_mul_f32 %0, %2, %0\n\tv_mul_f32 %1, %2, %1" : "+v"(sc[k]), "+v"(sc[k + 1]) : "v"(gv[0][mb]));
          else asm volatile("v_fmac_f32 %0, %2, %3\n\tv_fmac_f32 %1, %2, %4" : "+v"(sc[k]), "+v"(sc[k + 1]) : "v"(gn[mb]), "v"(ac2[nb][mb][e]), "v"(ac2[nb][mb][e + 1]));
          if constexpr (P >= 8 && e) own[nb][mb] = f32x4{sc[k - 2], sc[k - 1], sc[k], sc[k + 1]};
          SB();
        }
        if constexpr (P == FYK) { wait_vm<0>(); if (lane == 0) __hip_atomic_store(fy, tag, RLX_AGENT); SB(); }
        if constexpr (P == FYP) { asm volatile("global_load_dwordx2 %0, %1, off sc1" : "=v"(pv) : "v"(fz) : "memory"); SB(); }
        if constexpr (P == FYC) check();
      };
      auto sum_y = [&]() LAM {
        asm volatile("s_waitcnt vmcnt(0)" : "+v"(y[0][0]), "+v"(y[0][1]), "+v"(y[1][0]), "+v"(y[1][1]), "+v"(y[2][0]), "+v"(y[2][1]) :: "memory");
#pragma unroll
        for (int i = 0; i < 12; ++i) mfa(own[i & 1][(i >> 1) & 1], __builtin_bit_cast(bf16x8, pick[i & 1]), __builtin_bit_cast(bf16x8, y[i >> 2][(i >> 1) & 1]));
        settle();
#pragma unroll
        for (int i = 0; i < 4; ++i) pin(own[i & 1][i >> 1]);
        SB();
      };
      static_assert(FYK < FYP && FYP < FYC && FYC < ND, "stores, flag, sample, check");
      sfor<0, 6>([&](auto Dc) LAM {
        constexpr int d = decltype(Dc)::value, sl = d % 5;
        kstep(IC<d == 0>{}, 0, sl, 2); put(IC<4 * d>{}); kstep(IC<0>{}, 1, sl, 3); put(IC<4 * d + 1>{});
        BARRIER(); SB();
        kstep(IC<0>{}, 2, (sl + 1) % 5, 0); put(IC<4 * d + 2>{}); kstep(IC<0>{}, 3, (sl + 1) % 5, 1); put(IC<4 * d + 3>{});
      });
      kstep(IC<0>{}, 0, 1, 2); put(IC<24>{}); kstep(IC<0>{}, 1, 1, -1); put(IC<25>{}); kstep(IC<0>{}, 2, 1, -1); put(IC<26>{});
      settle();
#pragma unroll
      for (int i = 0; i < 4; ++i) { pin(acc[i >> 1][i & 1]); own[i >> 1][i & 1] += gn[i & 1] * acc[i >> 1][i & 1]; pin(own[i >> 1][i & 1]); }
      SB();
      if (late) { spin_flags(fz, fzl, tag); load_y(); }
      sum_y();
#pragma unroll
      for (int mb = 0; mb < 2; ++mb) {
        const f32x4 r0 = own[0][mb], r1 = own[1][mb];
        auto s0 = __builtin_amdgcn_permlane16_swap(cvtpk(r0[0], r0[1]), cvtpk(r1[0], r1[1]), false, false);
        auto s1 = __builtin_amdgcn_permlane16_swap(cvtpk(r0[2], r0[3]), cvtpk(r1[2], r1[3]), false, false);
        u32x4 v = {s0[0], s1[0], s0[1], s1[1]};
        SB();
        __builtin_amdgcn_raw_buffer_store_b128(v, rO, vO, mb * 4096, 16);
        sent();
      }
      asm volatile(".Lpf_end:");
    }
  }
#endif
}

extern "C" void kernel_launch(void* const* d_in, const int* in_sizes, int n_in, void* d_out, int out_size, void* d_ws,
                              size_t ws_size, hipStream_t stream) {
  (void)in_sizes; (void)n_in; (void)out_size; (void)ws_size;
  constexpr int kLds = 163840;
  (void)hipFuncSetAttribute((const void*)fused_kernel, hipFuncAttributeMaxDynamicSharedMemorySize, kLds);
  fused_kernel<<<256, 512, kLds, stream>>>((const u16*)d_in[0], (const u16*)d_in[1], (const u16*)d_in[2], (const u16*)d_in[3],
                                           (const u16*)d_in[4], (const u16*)d_in[6], (const u16*)d_in[7], (const u16*)d_in[8],
                                           (const u16*)d_in[9], (u16*)d_out, (char*)d_ws);
}
```
